```python
import torch

def _softmax(x, axis=-1):
    return torch.softmax(x, dim=axis)
def _gelu(x):
    return torch.nn.functional.gelu(x, approximate="tanh")
def _silu(x):
    return torch.nn.functional.silu(x)
def _sigmoid(x):
    return torch.sigmoid(x)
def _layernorm(x, axis=-1, eps=1e-5):
    m = x.mean(dim=axis, keepdim=True)
    v = x.var(dim=axis, keepdim=True, unbiased=False)
    return (x - m) / torch.sqrt(v + eps)
def _scatter_add(tgt, idx, src, axis=0):
    return tgt.clone().index_add_(axis, idx, src)
def _segment_sum(src, seg, n):
    out = torch.zeros((n,) + src.shape[1:], dtype=src.dtype)
    return out.index_add_(0, seg, src)
def _pad(x, pw):
    flat = tuple(v for pair in reversed(pw) for v in pair)
    return torch.nn.functional.pad(x, flat)

kernel_name = 'sequential_preLN__linear_attn_block__dense_activation_gelu__prompt'

def setup_inputs():

    _w = (lambda a: (lambda t: t.to(torch.bfloat16) if t.is_floating_point() else t)(torch.as_tensor(a)))
    def _A_setup():
        B, S, D = 1, 2048, 448
        return {
            "x": _w((lambda *s: torch.randn(*s, dtype=torch.bfloat16) * 0.05)(B, S, D)),
            "wqkv": _w((lambda *s: torch.randn(*s, dtype=torch.bfloat16) * 0.1)(D, 3 * D)),
            "wo": _w((lambda *s: torch.randn(*s, dtype=torch.bfloat16) * 0.05)(D, D)),
        }
    _A_in = _A_setup()

    def _B_setup():
        B, S, D, F = 1, 2048, 448, 1792
        return {
            "x": _w((lambda *s: torch.randn(*s, dtype=torch.bfloat16) * 0.05)(B, S, D)),
            "w1": _w((lambda *s: torch.randn(*s, dtype=torch.bfloat16) * 0.8)(D, F) * (D ** -0.5)),
            "b1": _w((lambda *s: torch.randn(*s, dtype=torch.bfloat16) * 12.8)(F) * 0.01),
            "w2": _w((lambda *s: torch.randn(*s, dtype=torch.bfloat16) * 0.4)(F, D) * (F ** -0.5)),
            "b2": _w((lambda *s: torch.randn(*s, dtype=torch.bfloat16) * 6.4)(D) * 0.01),
        }
    _B_in = _B_setup()

    return {
        "A_x": _A_in["x"],
        "A_wqkv": _A_in["wqkv"],
        "A_wo": _A_in["wo"],
        "B_x": _B_in["x"],
        "B_w1": _B_in["w1"],
        "B_b1": _B_in["b1"],
        "B_w2": _B_in["w2"],
        "B_b2": _B_in["b2"],
    }

def reference(A_x, A_wqkv, A_wo, B_x, B_w1, B_b1, B_w2, B_b2):
    def _A_ref(x, wqkv, wo):
        B, S, D = x.shape
        H = 16; Dh = D // H
        qkv = (lambda x, *d: x.permute(*d))((x @ wqkv).reshape(B, S, 3, H, Dh), 2, 0, 3, 1, 4)
        q, k, v = qkv[0], qkv[1], qkv[2]
        phi = lambda t: torch.where(t > 0, t + 1, torch.exp(t)) + 1e-6
        pq, pk = phi(q), phi(k)
        kv = torch.einsum("bhsd,bhse->bhde", pk, v)
        z = torch.einsum("bhsd->bhd", pk)
        num = torch.einsum("bhqd,bhde->bhqe", pq, kv)
        den = torch.einsum("bhqd,bhd->bhq", pq, z)[..., None] + 1e-6
        o = num / den
        return (lambda x, *d: x.permute(*d))(o, 0, 2, 1, 3).reshape(B, S, D) @ wo

    def _B_ref(x, w1, b1, w2, b2):
        h = torch.einsum("bsd,df->bsf", x, w1) + b1
        h = _gelu(h)
        return torch.einsum("bsf,fd->bsd", h, w2) + b2

    x = A_x
    h = x + _A_ref(_layernorm(x, axis=-1), A_wqkv, A_wo)
    h2 = h + _B_ref(_layernorm(h, axis=-1), B_w1, B_b1, B_w2, B_b2)
    return h2
```

```cpp
#include <hip/hip_runtime.h>
#include <hip/hip_bf16.h>
typedef __hip_bfloat16 bf16;
constexpr int S=2048, D=448, H=16, DH=28, F=1792;
__device__ inline float b2f(bf16 v){ return __bfloat162float(v);}
__device__ inline bf16 f2b(float v){ return __float2bfloat16(v);}

__global__ void k_ln(const bf16* x, bf16* y){
  int row = blockIdx.x; int l = threadIdx.x;
  float v[7]; float s=0;
  for(int i=0;i<7;i++){ v[i]=b2f(x[row*D+l+64*i]); s+=v[i]; }
  for(int o=32;o;o>>=1) s+=__shfl_xor(s,o);
  float m=s/D; float q=0;
  for(int i=0;i<7;i++){ float d=v[i]-m; q+=d*d; }
  for(int o=32;o;o>>=1) q+=__shfl_xor(q,o);
  float r=rsqrtf(q/D+1e-5f);
  for(int i=0;i<7;i++) y[row*D+l+64*i]=f2b((v[i]-m)*r);
}
template<int MODE>
__global__ void k_gemm(const bf16* A,const bf16* B,bf16* C,int M,int N,int K,const bf16* bias,const bf16* res){
  int n=blockIdx.x*64+threadIdx.x; int m0=blockIdx.y*8;
  if(n>=N) return;
  float acc[8]={0};
  for(int k=0;k<K;k++){ float b=b2f(B[k*N+n]);
    for(int i=0;i<8;i++) acc[i]+=b2f(A[(m0+i)*K+k])*b; }
  for(int i=0;i<8;i++){ float v=acc[i];
    if(MODE==1){ v+=b2f(res[(m0+i)*N+n]); }
    if(MODE==2){ v+=b2f(bias[n]); float u=0.7978845608028654f*(v+0.044715f*v*v*v); v=0.5f*v*(1.f+tanhf(u)); }
    if(MODE==3){ v+=b2f(bias[n]); v+=b2f(res[(m0+i)*N+n]); }
    C[(m0+i)*N+n]=f2b(v);}
}
__device__ inline float phi(float t){ return (t>0? t+1.f: __expf(t))+1e-6f; }
__global__ void k_kv(const bf16* qkv,float* kv){
  int h=blockIdx.x; int t=threadIdx.x; if(t>=DH*29) return; int d=t/29,e=t%29;
  float a=0; for(int s=0;s<S;s++){ float pk=b2f(f2b(phi(b2f(qkv[s*3*D+D+h*DH+d])))); float v= e<28? b2f(qkv[s*3*D+2*D+h*DH+e]):1.f; a+=pk*v; }
  kv[(h*DH+d)*29+e]=b2f(f2b(a));
}
__global__ void k_o(const bf16* qkv,const float* kv,bf16* o){
  int s=blockIdx.x; int t=threadIdx.x; if(t>=D) return; int h=t/DH,e=t%DH;
  float num=0,den=0; for(int d=0;d<DH;d++){ float pq=b2f(f2b(phi(b2f(qkv[s*3*D+h*DH+d])))); num+=pq*kv[(h*DH+d)*29+e]; den+=pq*kv[(h*DH+d)*29+28]; }
  o[s*D+t]=f2b(num/(den+1e-6f));
}
extern "C" void kernel_launch(void* const* d_in, const int* in_sizes, int n_in, void* d_out, int out_size, void* d_ws, size_t ws_size, hipStream_t stream){
  const bf16* x=(const bf16*)d_in[0]; const bf16* wqkv=(const bf16*)d_in[1]; const bf16* wo=(const bf16*)d_in[2];
  const bf16* w1=(const bf16*)d_in[4]; const bf16* b1=(const bf16*)d_in[5]; const bf16* w2=(const bf16*)d_in[6]; const bf16* b2=(const bf16*)d_in[7];
  char* ws=(char*)d_ws;
  bf16* xn=(bf16*)ws; bf16* qkv=(bf16*)(ws+(4<<20)); float* kv=(float*)(ws+(12<<20)); bf16* o=(bf16*)(ws+(13<<20)); bf16* h=(bf16*)(ws+(16<<20)); bf16* hn=(bf16*)(ws+(20<<20)); bf16* h1=(bf16*)(ws+(24<<20));
  k_ln<<<S,64,0,stream>>>(x,xn);
  k_gemm<0><<<dim3(3*D/64,S/8),64,0,stream>>>(xn,wqkv,qkv,S,3*D,D,nullptr,nullptr);
  k_kv<<<H,1024,0,stream>>>(qkv,kv);
  k_o<<<S,448,0,stream>>>(qkv,kv,o);
  k_gemm<1><<<dim3(D/64,S/8),64,0,stream>>>(o,wo,h,S,D,D,nullptr,x);
  k_ln<<<S,64,0,stream>>>(h,hn);
  k_gemm<2><<<dim3(F/64,S/8),64,0,stream>>>(hn,w1,h1,S,F,D,b1,nullptr);
  k_gemm<3><<<dim3(D/64,S/8),64,0,stream>>>(h1,w2,(bf16*)d_out,S,D,F,b2,h);
}
```

```cpp
#include <hip/hip_runtime.h>
#include <hip/hip_bf16.h>

#define LAS __attribute__((address_space(3)))
#define GAS __attribute__((address_space(1)))
#define DI __device__ __forceinline__
typedef unsigned v4u __attribute__((ext_vector_type(4)));
typedef unsigned v2u __attribute__((ext_vector_type(2)));
typedef float f32x4 __attribute__((ext_vector_type(4)));
typedef float f32x2 __attribute__((ext_vector_type(2)));
typedef short s16x4 __attribute__((ext_vector_type(4)));
typedef __bf16 bf16x8 __attribute__((ext_vector_type(8)));
typedef __bf16 bf16x2 __attribute__((ext_vector_type(2)));
typedef unsigned long long u64;
typedef unsigned short u16;
typedef __amdgpu_buffer_rsrc_t rsrc_t;

#define W2_N1 1
#define W2_N2 1
#define W2_N3 2

namespace {
constexpr int S_ = 2048, D_ = 448, F_ = 1792;
constexpr unsigned WS_EPOCH = 0;
constexpr unsigned WS_F1 = 4096;
constexpr unsigned WS_F2 = WS_F1 + 8192;
constexpr unsigned WS_F3 = WS_F2 + 16384;
constexpr unsigned WS_F4 = WS_F3 + 16384;
constexpr unsigned WS_KVP = 65536;
constexpr unsigned WS_O = WS_KVP + (1u << 20);
constexpr unsigned WS_H = WS_O + (2u << 20);
constexpr unsigned WS_ST = WS_H + (2u << 20);
constexpr unsigned WS_H1 = WS_ST + (256u << 10);
constexpr unsigned WS_END = WS_H1 + 128u * 56u * 1024u;
constexpr unsigned WS_DBG = 16u << 20;
constexpr unsigned K = 1024;
constexpr unsigned L_KV = 0, L_X = 40 * K, L_Q = 56 * K, L_WO = 88 * K, L_SC = 152 * K;
constexpr unsigned L_CS1 = L_SC, L_CSQ = L_SC + 512, L_STAT = L_SC + 1024, L_TAG = L_SC + 2048, L_C1 = L_SC + 2560;
constexpr unsigned L_EX = 64 * K, L_KVS = 80 * K;
constexpr unsigned L_W1 = 0, L_BB = 104 * K, L_PT = 0, L_HT = 140 * K;
constexpr unsigned LDS_BYTES = 163840;
constexpr int W1_EARLY = 9;
constexpr int AUX_SC1 = 16;
constexpr unsigned SPIN_CAP = 1u << 18;
constexpr unsigned xslot(int i) { return i == 0 || i == 6 ? 88 * K : i < 3 ? 40 * K + i * 16 * K : 56 * K + i * 16 * K; }

DI f32x4 mfma(v4u a, v4u b, f32x4 c) {
  return __builtin_amdgcn_mfma_f32_16x16x32_bf16(__builtin_bit_cast(bf16x8, a), __builtin_bit_cast(bf16x8, b), c, 0, 0, 0);
}
DI unsigned pack2(float a, float b) {
  bf16x2 v = {(__bf16)a, (__bf16)b};
  return __builtin_bit_cast(unsigned, v);
}
DI float asf(unsigned u) { return __builtin_bit_cast(float, u); }
DI unsigned asu(float f) { return __builtin_bit_cast(unsigned, f); }
DI float bflo(unsigned u) { return asf(u << 16); }
DI float bfhi(unsigned u) { return asf(u & 0xffff0000u); }
DI float dot2(unsigned a, unsigned b, float c) {
  return __builtin_amdgcn_fdot2_f32_bf16(__builtin_bit_cast(bf16x2, a), __builtin_bit_cast(bf16x2, b), c, false);
}
DI float gsum(float a) {
  auto r = __builtin_amdgcn_permlane16_swap(asu(a), asu(a), false, false);
  a = asf(r[0]) + asf(r[1]);
  auto q = __builtin_amdgcn_permlane32_swap(asu(a), asu(a), false, false);
  return asf(q[0]) + asf(q[1]);
}
DI v4u trfrag(LAS unsigned char* L, unsigned a, unsigned dt) {
  s16x4 lo = __builtin_amdgcn_ds_read_tr16_b64_v4i16((LAS s16x4*)(L + a));
  s16x4 hi = __builtin_amdgcn_ds_read_tr16_b64_v4i16((LAS s16x4*)(L + a + dt));
  v2u l2 = __builtin_bit_cast(v2u, lo), h2 = __builtin_bit_cast(v2u, hi);
  return v4u{l2.x, l2.y, h2.x, h2.y};
}
DI void dma16(rsrc_t rs, unsigned voff, unsigned soff, unsigned ldst) {
  asm volatile("s_mov_b32 m0, %0\n\ts_nop 0\n\tbuffer_load_dwordx4 %1, %2, %3 offen lds" ::"s"(ldst), "v"(voff), "s"(rs), "s"(soff) : "memory");
}
#define BAR() asm volatile("s_waitcnt lgkmcnt(0)\n\ts_barrier" ::: "memory")
#define BAR_VM() asm volatile("s_waitcnt vmcnt(0) lgkmcnt(0)\n\ts_barrier" ::: "memory")
#define DRAIN() asm volatile("s_waitcnt vmcnt(0)" ::: "memory")
#define SB() __builtin_amdgcn_sched_barrier(0)
DI float phi(float t) { return __builtin_amdgcn_exp2f(fminf(t, 0.f) * 1.4426950408889634f) + (fmaxf(t, 0.f) + 1e-6f); }
DI float gelu(float u) {
  const float k0 = -2.0f * 0.7978845608028654f * 1.4426950408889634f, k1 = k0 * 0.044715f;
  float e = __builtin_amdgcn_exp2f(u * __builtin_fmaf(u * u, k1, k0));
  return u * __builtin_amdgcn_rcpf(1.0f + e);
}
template <int N> DI void wait_flags(unsigned char* ws, unsigned off, u64 tag, int l) {
  const GAS u64* q = (const GAS u64*)(ws + off) + (l < N ? l : 0);
#pragma unroll 1
  for (unsigned it = 0; it < SPIN_CAP; ++it) {
    u64 v = __hip_atomic_load(q, __ATOMIC_RELAXED, __HIP_MEMORY_SCOPE_AGENT);
    if (__all(v == tag)) break;
    __builtin_amdgcn_s_sleep(1);
  }
  __builtin_amdgcn_fence(__ATOMIC_ACQUIRE, "wavefront");
}
DI void set_flag(unsigned char* ws, unsigned off, u64 tag, int l) {
  if (l == 0) __hip_atomic_store((GAS u64*)(ws + off), tag, __ATOMIC_RELAXED, __HIP_MEMORY_SCOPE_AGENT);
}
DI v4u ld_sc1(rsrc_t rs, unsigned voff, unsigned soff) { return __builtin_amdgcn_raw_buffer_load_b128(rs, voff, soff, AUX_SC1); }
DI v2u ld_sc1_64(rsrc_t rs, unsigned voff, unsigned soff) { return __builtin_amdgcn_raw_buffer_load_b64(rs, voff, soff, AUX_SC1); }
DI void st_sc1(v4u v, rsrc_t rs, unsigned voff, unsigned soff) { __builtin_amdgcn_raw_buffer_store_b128(v, rs, voff, soff, AUX_SC1); }
DI void st_sc1_64(v2u v, rsrc_t rs, unsigned voff, unsigned soff) { __builtin_amdgcn_raw_buffer_store_b64(v, rs, voff, soff, AUX_SC1); }

#define STAMP(k) do { } while (0)
}

__global__ void __launch_bounds__(512, 2)
fused_block(const u16* __restrict__ x, const u16* __restrict__ wqkv, const u16* __restrict__ wo, const u16* __restrict__ w1,
            const u16* __restrict__ b1, const u16* __restrict__ w2, const u16* __restrict__ b2, u16* __restrict__ out,
            unsigned char* ws) {
  __shared__ __attribute__((aligned(1024))) unsigned char smem[LDS_BYTES];
  LAS unsigned char* L = (LAS unsigned char*)smem;
  const unsigned lbase = (unsigned)(size_t)L;
  STAMP(0);
  const int tid = threadIdx.x, l = tid & 63, w = __builtin_amdgcn_readfirstlane(tid >> 6), g = l >> 4, c = l & 15;
  const int b = blockIdx.x, xcd = b & 7, slot = b >> 3;
  const int i1 = 4 * (xcd >> 1) + (slot >> 3), h = 8 * (xcd & 1) + (slot & 7);
  const int u = 4 * xcd + (slot >> 3), ct = slot & 7;
  const int i3 = 2 * xcd + (slot >> 4), c3 = slot & 15;
  const bool act24 = ct < 7;

  const rsrc_t rX = __builtin_amdgcn_make_buffer_rsrc((void*)x, 0, S_ * D_ * 2, 0x00020000);
  const rsrc_t rQ = __builtin_amdgcn_make_buffer_rsrc((void*)wqkv, 0, D_ * 3 * D_ * 2, 0x00020000);
  const rsrc_t rO = __builtin_amdgcn_make_buffer_rsrc((void*)wo, 0, D_ * D_ * 2, 0x00020000);
  const rsrc_t r1 = __builtin_amdgcn_make_buffer_rsrc((void*)w1, 0, D_ * F_ * 2, 0x00020000);
  const rsrc_t r2 = __builtin_amdgcn_make_buffer_rsrc((void*)w2, 0, F_ * D_ * 2, 0x00020000);
  const rsrc_t rW = __builtin_amdgcn_make_buffer_rsrc((void*)ws, 0, WS_END, 0x00020000);

  unsigned cpf;
  v2u ep;
  {
    u64 pc;
    unsigned sz;
    asm volatile(".Lfb_start:\n\ts_getpc_b64 %0\n\ts_mov_b32 %1, .Lfb_end-.Lfb_start" : "=s"(pc), "=s"(sz));
    const rsrc_t rC = __builtin_amdgcn_make_buffer_rsrc((void*)pc, 0, sz, 0x00020000);
    asm volatile("s_nop 4\n\tbuffer_load_dword %0, %1, %2, 0 offen" : "=v"(cpf) : "v"(tid * 128), "s"(rC) : "memory");
    asm volatile("buffer_load_dwordx2 %0, off, %1, 0 sc1" : "=v"(ep) : "s"(rW) : "memory");
  }

  const unsigned vW = (l >> 2) * 2688 + (((l >> 1) & 1) ^ (l >> 5)) * 24 + (l & 1) * 16;
  const unsigned vXa = (128 * i1 + 16 * w + (l >> 3)) * 896 + ((l & 7) ^ g) * 16;
  const unsigned vXb = (vXa ^ 64) + 8 * 896;
  {
    const unsigned sW = (32 * (w >> 2) + 16 * (w & 1)) * 2688 + (1 + ((w >> 1) & 1)) * 896 + 56 * h;
#pragma unroll
    for (int i = 0; i < 7; ++i) {
      dma16(rQ, vW, sW + i * 64 * 2688, lbase + L_KV + w * 1024 + i * 8192);
      if (i < 6) {
        dma16(rX, vXa, i * 128, lbase + xslot(i) + w * 2048);
        dma16(rX, vXb, i * 128, lbase + xslot(i) + w * 2048 + 1024);
      }
    }
  }
  STAMP(1);
  const unsigned lq0 = (l >> 5) * 1024 + (g & 1) * 544 + (c >> 2) * 64 + (c & 3) * 8, lq1 = lq0 ^ 32;
  const unsigned lx0 = w * 2048 + c * 128 + (g ^ (c >> 1)) * 16, lx1 = lx0 ^ 64;
  v4u xf[14];
  f32x4 acc[4];
  float s1 = 0.f, s2 = 0.f, csp = 0.f;
#pragma unroll
  for (int t = 0; t < 4; ++t) acc[t] = f32x4{0.f, 0.f, 0.f, 0.f};
  const unsigned ONES = 0x3F803F80u;
  const unsigned csa = (lq0 ^ ((w & 1) * 32)) + L_KV + (w >> 2) * 4096 + ((w >> 1) & 1) * 2048;
#pragma unroll
  for (int i = 0; i < 7; ++i) {
    constexpr int NW[7] = {16, 15, 12, 9, 10, 7, 4};
    asm volatile("s_waitcnt vmcnt(%3) lgkmcnt(0)\n\ts_barrier" : "+v"(s1), "+v"(s2), "+v"(csp) : "i"(NW[i]) : "memory");
    if (i == 3) {
#pragma unroll
      for (int m = 0; m < 4; ++m) {
        int n = w + 8 * m;
        if (m == 3) n = n >= 28 ? n - 4 : n;
        dma16(rQ, vW, n * 16 * 2688 + 56 * h, lbase + L_Q + n * 1024);
      }
    }
    xf[2 * i] = *(LAS v4u*)(L + xslot(i) + lx0);
    xf[2 * i + 1] = *(LAS v4u*)(L + xslot(i) + lx1);
    v4u f[8];
#pragma unroll
    for (int q = 0; q < 8; ++q) f[q] = trfrag(L, ((q & 1) ? lq1 : lq0) + L_KV + (2 * i + (q >> 2)) * 4096 + ((q >> 1) & 1) * 2048, 256);
    v4u fc = trfrag(L, csa + i * 8192, 256);
    if (i == 0) {
      asm volatile("s_waitcnt lgkmcnt(0)" : "+v"(xf[0]), "+v"(xf[1])::"memory");
      dma16(rX, vXa, 6 * 128, lbase + xslot(6) + w * 2048);
      dma16(rX, vXb, 6 * 128, lbase + xslot(6) + w * 2048 + 1024);
    }
    SB();
#pragma unroll
    for (int q = 0; q < 8; ++q) acc[q & 3] = mfma(xf[2 * i + (q >> 2)], f[q], acc[q & 3]);
#pragma unroll
    for (int ss = 0; ss < 2; ++ss)
#pragma unroll
      for (int d = 0; d < 4; ++d) {
        s1 = dot2(xf[2 * i + ss][d], ONES, s1);
        s2 = dot2(xf[2 * i + ss][d], xf[2 * i + ss][d], s2);
      }
#pragma unroll
    for (int d = 0; d < 4; ++d) csp = dot2(fc[d], ONES, csp);
  }
  STAMP(2);
  s1 = gsum(s1);
  s2 = gsum(s2);
  csp = gsum(csp);
  const float mean = s1 * (1.0f / 448.0f);
  const float rstd = __builtin_amdgcn_rsqf(fmaxf(s2 * (1.0f / 448.0f) - mean * mean, 0.f) + 1e-5f);
  const float nmr = -mean * rstd;
  if (g == 0) {
    *(LAS float*)(L + L_CS1 + w * 64 + c * 4) = csp;
    *(LAS f32x2*)(L + L_STAT + w * 128 + c * 8) = f32x2{rstd, nmr};
  }
  BAR();
  const unsigned v8 = (l >> 3) * 896 + (((l >> 1) & 3) ^ g) * 32 + (l & 1) * 16 + ct * 128;
  if (w >= 4 && act24) {
#pragma unroll
    for (int m = 0; m < 16; ++m) {
      const int n = 16 * (w - 4) + m;
      dma16(rO, v8, (28 * (n >> 2) + 12 * ((m >> 1) & 1) + 8 * (m & 1)) * 896, lbase + L_WO + n * 1024);
    }
  }
  {
    f32x4 st0 = *(LAS f32x4*)(L + L_STAT + w * 128 + g * 32);
    f32x4 st1 = *(LAS f32x4*)(L + L_STAT + w * 128 + g * 32 + 16);
    const float rs[4] = {st0.x, st0.z, st1.x, st1.z}, nm[4] = {st0.y, st0.w, st1.y, st1.w};
#pragma unroll
    for (int t = 0; t < 4; ++t) {
      const float cs = *(LAS float*)(L + L_CS1 + t * 64 + c * 4) + *(LAS float*)(L + L_CS1 + (t + 4) * 64 + c * 4);
      float v[4];
#pragma unroll
      for (int r = 0; r < 4; ++r) {
        v[r] = __builtin_fmaf(acc[t][r], rs[r], nm[r] * cs);
        if (t < 2) v[r] = phi(v[r]);
        if (t == 3) v[r] = c < 4 ? 1.0f : v[r];
      }
      *(LAS v2u*)(L + L_X + ((t * 4 + (w >> 1)) * 64 + l) * 16 + (w & 1) * 8) = v2u{pack2(v[0], v[1]), pack2(v[2], v[3])};
    }
  }
  if (w < 4 || !act24) {
    asm volatile("s_waitcnt vmcnt(0)" : "+v"(ep), "+v"(cpf)::"memory");
    if (tid == 0) *(LAS u64*)(L + L_TAG) = (((u64)ep.y << 32) | ep.x) * 6364136223846793005ull + 1442695040888963407ull;
    BAR();
  } else {
    asm volatile("s_waitcnt vmcnt(16) lgkmcnt(0)\n\ts_barrier" : "+v"(ep), "+v"(cpf)::"memory");
  }
  const u64 tag = *(LAS u64*)(L + L_TAG);
  STAMP(3);
  if (w < 4) {
    f32x4 kv = {0.f, 0.f, 0.f, 0.f};
    v4u a[4], bb[4];
#pragma unroll
    for (int ks = 0; ks < 4; ++ks) {
      a[ks] = *(LAS v4u*)(L + L_X + (((w >> 1) * 4 + ks) * 64 + l) * 16);
      bb[ks] = *(LAS v4u*)(L + L_X + (((2 + (w & 1)) * 4 + ks) * 64 + l) * 16);
    }
#pragma unroll
    for (int ks = 0; ks < 4; ++ks) kv = mfma(a[ks], bb[ks], kv);
    if (w >= 2 && g == 0) kv = f32x4{0.f, 0.f, 0.f, 0.f};
    st_sc1(__builtin_bit_cast(v4u, kv), rW, l * 16, WS_KVP + ((h * 4 + w) * 16 + i1) * 1024);
    DRAIN();
    set_flag(ws, WS_F1 + ((h * 4 + w) * 16 + i1) * 8, tag, l);
  }
  STAMP(4);
  v4u w2r[7][4];
  auto w2load = [&](int kk) {
#pragma unroll
    for (int n = 0; n < 4; ++n) w2r[kk][n] = __builtin_amdgcn_raw_buffer_load_b128(r2, v8, (224 * w + 32 * kk + 8 * n) * 896, 0);
  };
  if (act24) {
#pragma unroll
    for (int kk = 0; kk < W2_N1; ++kk) w2load(kk);
  }
  SB();
  v4u pqf;
  {
    f32x4 aq[2] = {f32x4{0.f, 0.f, 0.f, 0.f}, f32x4{0.f, 0.f, 0.f, 0.f}};
    float csq = 0.f;
    const unsigned qa = (lq0 ^ ((w & 1) * 32)) + L_Q + (w >> 1) * 2048;
    v4u fc[4];
#pragma unroll
    for (int m = 0; m < 4; ++m) fc[m] = trfrag(L, qa + (m < 3 ? m * 8192 : (w < 4 ? 3 * 8192 : 0)), 256);
    v4u f[28];
#pragma unroll
    for (int q = 0; q < 6; ++q) f[q] = trfrag(L, ((q & 1) ? lq1 : lq0) + L_Q + (q >> 1) * 2048, 256);
#pragma unroll
    for (int m = 0; m < 4; ++m) {
      float t = 0.f;
#pragma unroll
      for (int d = 0; d < 4; ++d) t = dot2(fc[m][d], ONES, t);
      csq += (m < 3 || w < 4) ? t : 0.f;
    }
    csq = gsum(csq);
    if (g == 0) *(LAS float*)(L + L_CSQ + w * 64 + c * 4) = csq;
    SB();
#pragma unroll
    for (int q = 0; q < 28; ++q) {
      if (q + 6 < 28) f[q + 6] = trfrag(L, ((q & 1) ? lq1 : lq0) + L_Q + ((q + 6) >> 1) * 2048, 256);
      aq[q & 1] = mfma(f[q], xf[q >> 1], aq[q & 1]);
      SB();
    }
    BAR();
#pragma unroll
    for (int t = 0; t < 2; ++t) {
      f32x4 cs = *(LAS f32x4*)(L + L_CSQ + t * 64 + g * 16);
#pragma unroll
      for (int m = 1; m < 4; ++m) cs += *(LAS f32x4*)(L + L_CSQ + (t + 2 * m) * 64 + g * 16);
      float v[4];
#pragma unroll
      for (int r = 0; r < 4; ++r) v[r] = phi(__builtin_fmaf(aq[t][r], rstd, nmr * cs[r]));
      pqf[2 * t] = pack2(v[0], v[1]);
      pqf[2 * t + 1] = pack2(v[2], v[3]);
    }
  }
  STAMP(5);
  wait_flags<8>(ws, WS_F1 + ((h * 4 + (w & 3)) * 16 + 8 * (w >> 2)) * 8, tag, l);
  asm volatile("" ::"v"(cpf), "v"(ep));
  STAMP(6);
  {
    v4u p[8];
#pragma unroll
    for (int n = 0; n < 8; ++n) p[n] = ld_sc1(rW, l * 16 + (n & 3) * 1024, WS_KVP + ((h * 4 + (w & 3)) * 16 + 8 * (w >> 2) + (n & 4)) * 1024);
    SB();
    f32x4 sum = __builtin_bit_cast(f32x4, p[0]);
#pragma unroll
    for (int n = 1; n < 8; ++n) sum += __builtin_bit_cast(f32x4, p[n]);
    *(LAS f32x4*)(L + L_KVS + w * 1024 + l * 16) = sum;
  }
  BAR();
  STAMP(7);
  {
    f32x4 T[4], Z[2];
#pragma unroll
    for (int dt = 0; dt < 2; ++dt)
      Z[dt] = *(LAS f32x4*)(L + L_KVS + (dt * 2 + 1) * 1024 + g * 256) + *(LAS f32x4*)(L + L_KVS + (dt * 2 + 5) * 1024 + g * 256);
#pragma unroll
    for (int t = 0; t < 4; ++t) T[t] = *(LAS f32x4*)(L + L_KVS + t * 1024 + l * 16) + *(LAS f32x4*)(L + L_KVS + (t + 4) * 1024 + l * 16);
    v4u f0 = {pack2(T[0].x, T[0].y), pack2(T[0].z, T[0].w), pack2(T[2].x, T[2].y), pack2(T[2].z, T[2].w)};
    v4u f1 = {pack2(T[1].x, T[1].y), pack2(T[1].z, T[1].w), pack2(T[3].x, T[3].y), pack2(T[3].z, T[3].w)};
    v4u fz = {pack2(Z[0].x, Z[0].y), pack2(Z[0].z, Z[0].w), pack2(Z[1].x, Z[1].y), pack2(Z[1].z, Z[1].w)};
    const f32x4 z4 = {0.f, 0.f, 0.f, 0.f};
    f32x4 den = mfma(fz, pqf, z4), n0 = mfma(f0, pqf, z4), n1 = mfma(f1, pqf, z4);
    const float rd = __builtin_amdgcn_rcpf(den.x + 1e-6f);
    const float rd1 = g == 0 ? 0.f : rd;
    v4u of = {pack2(n0.x * rd, n0.y * rd), pack2(n0.z * rd, n0.w * rd), pack2(n1.x * rd1, n1.y * rd1), pack2(n1.z * rd1, n1.w * rd1)};
    st_sc1(of, rW, l * 16, WS_O + ((8 * i1 + w) * 16 + h) * 1024);
    DRAIN();
    set_flag(ws, WS_F2 + ((8 * i1 + w) * 16 + h) * 8, tag, l);
  }
  STAMP(8);
  unsigned v1off;
  {
    const int ch = 64 * w + l, row = (ch * 2341) >> 15, cc = ch - 14 * row, js = cc >> 1;
    const int j = (c3 & 1) ? (js == 6 ? 0 : js + 1) : js;
    v1off = row * 3584 + (112 * c3 + 16 * j) * 2 + (cc & 1) * 16;
  }
  if (w < 7) {
#pragma unroll
    for (int s = 0; s < W1_EARLY; ++s) dma16(r1, v1off, s * 32 * 3584, lbase + L_W1 + s * 7168 + w * 1024);
  }
  const int mt = w & 3, kh = w >> 2;
  const unsigned l8 = (l >> 5) * 1024 + ((g & 1) * 4 + (c >> 2)) * 128 + ((g & 1) * 2 + (c >> 3)) * 32 + (c & 3) * 8;
  v4u hres;
  if (act24) {
#pragma unroll
    for (int kk = W2_N1; kk < W2_N1 + W2_N2; ++kk) w2load(kk);
    const unsigned vxr = (64 * u + 16 * mt + c) * 896 + (64 * ct + 32 * kh + 4 * g) * 2;
    v2u xr0 = __builtin_amdgcn_raw_buffer_load_b64(rX, vxr, 0, 0), xr1 = __builtin_amdgcn_raw_buffer_load_b64(rX, vxr + 32, 0, 0);
    const unsigned wb = (l8 ^ (kh * 64)) + L_WO + kh * 32768;
    v4u wf[32];
    constexpr int PF2 = 8;
#pragma unroll
    for (int q = 0; q < PF2; ++q) wf[q] = trfrag(L, (wb ^ ((q & 3) * 32)) + (q >> 2) * 4096, 2048);
    STAMP(9);
    const int m2 = 4 * u + mt;
    wait_flags<8>(ws, WS_F2 + (m2 * 16 + 8 * kh) * 8, tag, l);
    STAMP(10);
    v4u of[8];
#pragma unroll
    for (int n = 0; n < 8; ++n) of[n] = ld_sc1(rW, l * 16 + (n & 3) * 1024, WS_O + (m2 * 16 + 8 * kh + (n & 4)) * 1024);
    SB();
    f32x4 a2[4];
#pragma unroll
    for (int j = 0; j < 4; ++j) a2[j] = f32x4{0.f, 0.f, 0.f, 0.f};
#pragma unroll
    for (int q = 0; q < 32; ++q) {
      if (q + PF2 < 32) wf[q + PF2] = trfrag(L, (wb ^ ((q & 3) * 32)) + ((q + PF2) >> 2) * 4096, 2048);
      a2[q & 3] = mfma(wf[q], of[q >> 2], a2[q & 3]);
      SB();
    }
    *(LAS f32x4*)(L + L_EX + w * 2048 + l * 16) = a2[2];
    *(LAS f32x4*)(L + L_EX + w * 2048 + 1024 + l * 16) = a2[3];
    BAR();
    STAMP(11);
    a2[0] += *(LAS f32x4*)(L + L_EX + (w ^ 4) * 2048 + l * 16);
    a2[1] += *(LAS f32x4*)(L + L_EX + (w ^ 4) * 2048 + 1024 + l * 16);
    hres = v4u{pack2(a2[0].x + bflo(xr0.x), a2[0].y + bfhi(xr0.x)), pack2(a2[0].z + bflo(xr0.y), a2[0].w + bfhi(xr0.y)),
               pack2(a2[1].x + bflo(xr1.x), a2[1].y + bfhi(xr1.x)), pack2(a2[1].z + bflo(xr1.y), a2[1].w + bfhi(xr1.y))};
    st_sc1(hres, rW, l * 16, WS_H + (m2 * 16 + 2 * ct + kh) * 1024);
    float t1 = 0.f, t2 = 0.f;
#pragma unroll
    for (int d = 0; d < 4; ++d) {
      t1 = dot2(hres[d], ONES, t1);
      t2 = dot2(hres[d], hres[d], t2);
    }
    t1 = gsum(t1);
    t2 = gsum(t2);
    if (g == 0) st_sc1_64(v2u{asu(t1), asu(t2)}, rW, c * 8, WS_ST + (m2 * 16 + 2 * ct + kh) * 128);
    DRAIN();
    set_flag(ws, WS_F3 + (m2 * 16 + 2 * ct + kh) * 8, tag, l);
    if (b == 0 && tid == 0) __hip_atomic_store((GAS u64*)(ws + WS_EPOCH), tag, __ATOMIC_RELAXED, __HIP_MEMORY_SCOPE_AGENT);
  }
  STAMP(12);
  BAR();
  if (w < 7) {
#pragma unroll
    for (int s = W1_EARLY; s < 14; ++s) dma16(r1, v1off, s * 32 * 3584, lbase + L_W1 + s * 7168 + w * 1024);
  }
  BAR_VM();
  const unsigned lP3 = ((l >> 5) * 8 + (g & 1) * 4 + (c >> 2)) * 224 + (c & 3) * 8 + L_W1;
  if (act24) {
#pragma unroll
    for (int kk = W2_N1 + W2_N2; kk < W2_N1 + W2_N2 + W2_N3; ++kk) w2load(kk);
  }
  if (w < 7) {
    const int j = (c3 & 1) ? (w == 6 ? 0 : w + 1) : w;
    v2u bb = *(const GAS v2u*)(b1 + 112 * c3 + 16 * j + 4 * g);
    f32x4 cs = {0.f, 0.f, 0.f, 0.f};
    const v4u on = {ONES, ONES, ONES, ONES};
    v4u f[14];
#pragma unroll
    for (int s = 0; s < 14; ++s) f[s] = trfrag(L, lP3 + w * 32 + s * 7168, 3584);
#pragma unroll
    for (int s = 0; s < 14; ++s) cs = mfma(f[s], on, cs);
    if (c == 0) {
      *(LAS f32x4*)(L + L_C1 + w * 64 + g * 16) = cs;
      *(LAS f32x4*)(L + L_C1 + 448 + w * 64 + g * 16) = f32x4{bflo(bb.x), bfhi(bb.x), bflo(bb.y), bfhi(bb.y)};
    }
  }
  BAR();
  STAMP(13);
  const int m3 = 8 * i3 + w;
  wait_flags<14>(ws, WS_F3 + m3 * 128, tag, l);
  STAMP(14);
  {
    v4u hf[14];
#pragma unroll
    for (int s = 0; s < 14; ++s) {
      hf[s] = ld_sc1(rW, l * 16 + (s & 3) * 1024, WS_H + (m3 * 16 + (s & ~3)) * 1024);
      SB();
    }
    v2u st[4];
#pragma unroll
    for (int m = 0; m < 4; ++m) st[m] = ld_sc1_64(rW, c * 8 + g * 128 + m * 512, WS_ST + m3 * 2048);
    if (act24) {
#pragma unroll
      for (int kk = W2_N1 + W2_N2 + W2_N3; kk < 7; ++kk) w2load(kk);
    }
    SB();
    f32x4 a3[7];
#pragma unroll
    for (int j = 0; j < 7; ++j) a3[j] = f32x4{0.f, 0.f, 0.f, 0.f};
    auto fs = [](int q) { return q < 56 ? q >> 2 : (q - 56) % 14; };
    auto fj = [](int q) { return q < 56 ? q & 3 : 4 + (q - 56) / 14; };
    constexpr int PF3 = 6;
    v4u f[98];
#pragma unroll
    for (int q = 0; q < PF3; ++q) f[q] = trfrag(L, lP3 + fs(q) * 7168 + fj(q) * 32, 3584);
    SB();
#pragma unroll
    for (int q = 0; q < 56; ++q) {
      f[q + PF3] = trfrag(L, lP3 + fs(q + PF3) * 7168 + fj(q + PF3) * 32, 3584);
      a3[fj(q)] = mfma(f[q], hf[fs(q)], a3[fj(q)]);
      SB();
    }
    float t1 = 0.f, t2 = 0.f;
#pragma unroll
    for (int m = 0; m < 4; ++m) {
      const bool ok = m < 3 || g < 2;
      t1 += ok ? asf(st[m].x) : 0.f;
      t2 += ok ? asf(st[m].y) : 0.f;
    }
    t1 = gsum(t1);
    t2 = gsum(t2);
    const float mean2 = t1 * (1.0f / 448.0f);
    const float rs2 = __builtin_amdgcn_rsqf(fmaxf(t2 * (1.0f / 448.0f) - mean2 * mean2, 0.f) + 1e-5f);
    const float nm2 = -mean2 * rs2;
    const unsigned ksp = (7 * c3 + 1) >> 1;
    const unsigned ob = WS_H1 + (m3 * 56 + ksp) * 1024;
    unsigned pk[14];
    auto epi = [&](int j) {
      f32x4 cs = *(LAS f32x4*)(L + L_C1 + j * 64 + g * 16);
      f32x4 bb = *(LAS f32x4*)(L + L_C1 + 448 + j * 64 + g * 16);
      float y[4];
#pragma unroll
      for (int r = 0; r < 4; ++r) y[r] = gelu(__builtin_fmaf(a3[j][r], rs2, __builtin_fmaf(nm2, cs[r], bb[r])));
      pk[2 * j] = pack2(y[0], y[1]);
      pk[2 * j + 1] = pack2(y[2], y[3]);
    };
    SB();
#pragma unroll
    for (int j = 4; j < 7; ++j) {
#pragma unroll
      for (int hh = 0; hh < 2; ++hh) {
#pragma unroll
        for (int s = 7 * hh; s < 7 * hh + 7; ++s) {
          const int q = 56 + (j - 4) * 14 + s;
          if (q + PF3 < 98) f[q + PF3] = trfrag(L, lP3 + fs(q + PF3) * 7168 + fj(q + PF3) * 32, 3584);
          a3[j] = mfma(f[q], hf[s], a3[j]);
        }
        epi(2 * (j - 4) + hh);
        if (hh == 1) st_sc1(v4u{pk[4 * (j - 4)], pk[4 * (j - 4) + 1], pk[4 * (j - 4) + 2], pk[4 * (j - 4) + 3]}, rW, l * 16 + (j - 4) * 1024, ob);
        SB();
      }
    }
    epi(6);
    st_sc1_64(v2u{pk[12], pk[13]}, rW, l * 8, (c3 & 1) ? ob - 512 : ob + 3072);
    DRAIN();
    set_flag(ws, WS_F4 + (((m3 >> 2) * 16 + c3) * 4 + (m3 & 3)) * 8, tag, l);
  }
  STAMP(15);
  if (act24) {
    v4u wf[7][4];
#pragma unroll
    for (int kk = 0; kk < 7; ++kk) {
#pragma unroll
      for (int n = 0; n < 4; ++n) *(LAS v4u*)(L + L_BB + w * 4096 + n * 1024 + l * 16) = w2r[kk][n];
      SB();
#pragma unroll
      for (int j = 0; j < 4; ++j) wf[kk][j] = trfrag(L, (l8 ^ (j * 32)) + L_BB + w * 4096, 2048);
      SB();
    }
    v4u bb2 = *(const GAS v4u*)(b2 + 64 * ct + (tid & 7) * 8);
    BAR();
    *(LAS v2u*)(L + L_HT + (16 * mt + c) * 144 + (32 * kh + 4 * g) * 2) = v2u{hres.x, hres.y};
    *(LAS v2u*)(L + L_HT + (16 * mt + c) * 144 + (32 * kh + 16 + 4 * g) * 2) = v2u{hres.z, hres.w};
    STAMP(16);
    wait_flags<8>(ws, WS_F4 + (u * 16 + 2 * w) * 32, tag, l);
    STAMP(17);
    v4u hf[4][7];
#pragma unroll
    for (int m = 0; m < 4; ++m) {
      const unsigned so = WS_H1 + ((4 * u + m) * 56 + 7 * w) * 1024;
#pragma unroll
      for (int kk = 0; kk < 7; ++kk) {
        if (kk == 3) {
          v2u lo = ld_sc1_64(rW, l * 8 + 3072, so), hi = ld_sc1_64(rW, l * 8 + 3584, so);
          hf[m][kk] = v4u{lo.x, lo.y, hi.x, hi.y};
        } else if (kk < 3) {
          hf[m][kk] = ld_sc1(rW, l * 16 + kk * 1024, so);
        } else {
          hf[m][kk] = ld_sc1(rW, l * 16 + (kk - 4) * 1024, so + 4096);
        }
        SB();
      }
    }
#pragma unroll
    for (int m = 0; m < 4; ++m) {
      f32x4 a4[4];
#pragma unroll
      for (int j = 0; j < 4; ++j) a4[j] = f32x4{0.f, 0.f, 0.f, 0.f};
#pragma unroll
      for (int kk = 0; kk < 7; ++kk)
#pragma unroll
        for (int j = 0; j < 4; ++j) a4[j] = mfma(wf[kk][j], hf[m][kk], a4[j]);
#pragma unroll
      for (int j = 0; j < 4; ++j) *(LAS f32x4*)(L + L_PT + w * 17408 + (16 * m + c) * 272 + (16 * j + 4 * g) * 4) = a4[j];
      SB();
    }
    BAR();
    STAMP(18);
    const unsigned pa = L_PT + (tid >> 3) * 272 + (tid & 7) * 32;
    f32x4 o0 = *(LAS f32x4*)(L + pa), o1 = *(LAS f32x4*)(L + pa + 16);
#pragma unroll
    for (int ww = 1; ww < 8; ++ww) {
      o0 += *(LAS f32x4*)(L + pa + ww * 17408);
      o1 += *(LAS f32x4*)(L + pa + ww * 17408 + 16);
    }
    v4u hh = *(LAS v4u*)(L + L_HT + (tid >> 3) * 144 + (tid & 7) * 16);
    v4u ov = {pack2(o0.x + bflo(bb2.x) + bflo(hh.x), o0.y + bfhi(bb2.x) + bfhi(hh.x)),
              pack2(o0.z + bflo(bb2.y) + bflo(hh.y), o0.w + bfhi(bb2.y) + bfhi(hh.y)),
              pack2(o1.x + bflo(bb2.z) + bflo(hh.z), o1.y + bfhi(bb2.z) + bfhi(hh.z)),
              pack2(o1.z + bflo(bb2.w) + bflo(hh.w), o1.w + bfhi(bb2.w) + bfhi(hh.w))};
    *(GAS v4u*)(out + (64 * u + (tid >> 3)) * 448 + 64 * ct + (tid & 7) * 8) = ov;
  }
  STAMP(19);
  asm volatile(".Lfb_end:" ::: "memory");
}

extern "C" void kernel_launch(void* const* d_in, const int* in_sizes, int n_in, void* d_out, int out_size, void* d_ws,
                              size_t ws_size, hipStream_t stream) {
  (void)in_sizes; (void)n_in; (void)out_size; (void)ws_size;
  fused_block<<<256, 512, 0, stream>>>((const u16*)d_in[0], (const u16*)d_in[1], (const u16*)d_in[2], (const u16*)d_in[4],
                                       (const u16*)d_in[5], (const u16*)d_in[6], (const u16*)d_in[7], (u16*)d_out,
                                       (unsigned char*)d_ws);
}
```

```cpp
#include <hip/hip_runtime.h>
#include <hip/hip_bf16.h>

#define LAS __attribute__((address_space(3)))
#define GAS __attribute__((address_space(1)))
#define DI __device__ __forceinline__
typedef unsigned v4u __attribute__((ext_vector_type(4)));
typedef unsigned v2u __attribute__((ext_vector_type(2)));
typedef float f32x4 __attribute__((ext_vector_type(4)));
typedef float f32x2 __attribute__((ext_vector_type(2)));
typedef short s16x4 __attribute__((ext_vector_type(4)));
typedef __bf16 bf16x8 __attribute__((ext_vector_type(8)));
typedef __bf16 bf16x2 __attribute__((ext_vector_type(2)));
typedef unsigned long long u64;
typedef unsigned short u16;
typedef __amdgpu_buffer_rsrc_t rsrc_t;

#define W2_A 12
#define W2_B 14

namespace {
constexpr int S_ = 2048, D_ = 448, F_ = 1792;
constexpr unsigned FS = 64;
constexpr unsigned WS_EPOCH = 0;
constexpr unsigned WS_F1 = 4096;
constexpr unsigned WS_F2 = WS_F1 + 256 * FS;
constexpr unsigned WS_F3 = WS_F2 + 256 * FS;
constexpr unsigned WS_F4 = WS_F3 + 256 * FS;
constexpr unsigned WS_FR = WS_F4 + 256 * FS;
constexpr unsigned WS_KVP = 131072;
constexpr unsigned WS_O = WS_KVP + (1u << 20);
constexpr unsigned WS_H = WS_O + (2u << 20);
constexpr unsigned WS_ST = WS_H + (2u << 20);
constexpr unsigned WS_H1 = WS_ST + (256u << 10);
constexpr unsigned WS_R1 = WS_H1 + 128u * 56u * 1024u;
constexpr unsigned WS_R2 = WS_R1 + 16u * 14u * 7168u;
constexpr unsigned WS_END = WS_R2 + 7u * 56u * 4096u;
constexpr unsigned WS_DBG = 32u << 20;
constexpr unsigned K = 1024;
constexpr unsigned L_KV = 0, L_Q = 56 * K, L_X = 136 * K, L_WO = 98 * K, L_SC = 156 * K;
constexpr unsigned L_CSQ = L_SC, L_CS1 = L_SC + 512, L_C1 = L_SC + 1024;
constexpr unsigned L_EX = 100 * K, L_KVS = 88 * K, L_RP = 56 * K;
constexpr unsigned L_W1 = 0, L_PT = 0, L_HT = 140 * K;
constexpr unsigned LDS_BYTES = 163840;
constexpr int AUX_SC1 = 16;
constexpr unsigned SPIN_CAP = 1u << 18;
constexpr unsigned xslot(int i) { return 88 * K + (i & 3) * 16 * K; }

DI f32x4 mfma(v4u a, v4u b, f32x4 c) {
  return __builtin_amdgcn_mfma_f32_16x16x32_bf16(__builtin_bit_cast(bf16x8, a), __builtin_bit_cast(bf16x8, b), c, 0, 0, 0);
}
DI f32x4 rowsum(float v) {
  asm volatile("s_nop 2" : "+v"(v));
  return __builtin_amdgcn_mfma_f32_16x16x4f32(v, 1.0f, f32x4{0.f, 0.f, 0.f, 0.f}, 0, 0, 0);
}
DI float colsum(float v) {
  asm volatile("s_nop 2" : "+v"(v));
  return __builtin_amdgcn_mfma_f32_16x16x4f32(1.0f, v, f32x4{0.f, 0.f, 0.f, 0.f}, 0, 0, 0)[0];
}
DI unsigned pack2(float a, float b) {
  bf16x2 v = {(__bf16)a, (__bf16)b};
  return __builtin_bit_cast(unsigned, v);
}
DI float asf(unsigned u) { return __builtin_bit_cast(float, u); }
DI unsigned asu(float f) { return __builtin_bit_cast(unsigned, f); }
DI float bflo(unsigned u) { return asf(u << 16); }
DI float bfhi(unsigned u) { return asf(u & 0xffff0000u); }
DI float dot2(unsigned a, unsigned b, float c) {
  return __builtin_amdgcn_fdot2_f32_bf16(__builtin_bit_cast(bf16x2, a), __builtin_bit_cast(bf16x2, b), c, false);
}
DI v4u trfrag2(LAS unsigned char* L, unsigned a, unsigned b) {
  s16x4 lo = __builtin_amdgcn_ds_read_tr16_b64_v4i16((LAS s16x4*)(L + a));
  s16x4 hi = __builtin_amdgcn_ds_read_tr16_b64_v4i16((LAS s16x4*)(L + b));
  v2u l2 = __builtin_bit_cast(v2u, lo), h2 = __builtin_bit_cast(v2u, hi);
  return v4u{l2.x, l2.y, h2.x, h2.y};
}
DI v4u trfrag(LAS unsigned char* L, unsigned a, unsigned dt) { return trfrag2(L, a, a + dt); }
DI void dma16(rsrc_t rs, unsigned voff, unsigned soff, unsigned ldst) {
  asm volatile("s_mov_b32 m0, %0\n\ts_nop 0\n\tbuffer_load_dwordx4 %1, %2, %3 offen lds" ::"s"(ldst), "v"(voff), "s"(rs), "s"(soff) : "memory");
}
DI void dma16s(rsrc_t rs, unsigned voff, unsigned soff, unsigned ldst) {
  asm volatile("s_mov_b32 m0, %0\n\ts_nop 0\n\tbuffer_load_dwordx4 %1, %2, %3 offen sc1 lds" ::"s"(ldst), "v"(voff), "s"(rs), "s"(soff) : "memory");
}
DI void dma16x2(rsrc_t rs, unsigned voff, unsigned voff2, unsigned soff, unsigned ldst) {
  asm volatile("s_mov_b32 m0, %0\n\ts_nop 0\n\tbuffer_load_dwordx4 %1, %3, %4 offen lds\n\tbuffer_load_dwordx4 %2, %3, %4 offen offset:1024 lds"
               ::"s"(ldst), "v"(voff), "v"(voff2), "s"(rs), "s"(soff) : "memory");
}
template <int STR> DI void dma16x4(rsrc_t rs, unsigned voff, unsigned soff, unsigned ldst) {
  unsigned t;
  asm volatile("s_mov_b32 m0, %1\n\ts_add_u32 %0, %4, %5\n\tbuffer_load_dwordx4 %2, %3, %4 offen lds\n\t"
               "buffer_load_dwordx4 %2, %3, %0 offen offset:1024 lds\n\ts_add_u32 %0, %0, %5\n\t"
               "buffer_load_dwordx4 %2, %3, %0 offen offset:2048 lds\n\ts_add_u32 %0, %0, %5\n\t"
               "buffer_load_dwordx4 %2, %3, %0 offen offset:3072 lds"
               : "=&s"(t) : "s"(ldst), "v"(voff), "s"(rs), "s"(soff), "i"(STR - 1024) : "memory", "scc");
}
#define BAR() asm volatile("s_waitcnt lgkmcnt(0)\n\ts_barrier" ::: "memory")
#define BAR_VM() asm volatile("s_waitcnt vmcnt(0) lgkmcnt(0)\n\ts_barrier" ::: "memory")
#define DRAIN() asm volatile("s_waitcnt vmcnt(0)" ::: "memory")
#define SB() __builtin_amdgcn_sched_barrier(0)
#define OPQ(x) asm("" : "+v"(x))
constexpr float LOG2E = 1.4426950408889634f;
DI float phi(float t, float rs, float rl) { return __builtin_amdgcn_exp2f(fminf(t * rl, 0.f)) + fmaxf(__builtin_fmaf(t, rs, 1e-6f), 1e-6f); }
constexpr float GC = 0.4686686930532063f, GK = -4.91222953926424f;
DI float gelu_s(float v) {
  float e = __builtin_amdgcn_exp2f(v * __builtin_fmaf(-v, v, GK));
  float y = v * __builtin_amdgcn_rcpf(1.0f + e);
  OPQ(y);
  return y;
}
DI void poll_flags(unsigned char* ws, unsigned off, int idx, u64 tag) {
  const GAS u64* q = (const GAS u64*)(ws + off + idx * FS);
#pragma unroll 1
  for (unsigned it = 0; it < SPIN_CAP; ++it) {
    u64 v = __hip_atomic_load(q, __ATOMIC_RELAXED, __HIP_MEMORY_SCOPE_AGENT);
    if (__all(v == tag)) break;
    __builtin_amdgcn_s_sleep(1);
  }
  __builtin_amdgcn_fence(__ATOMIC_ACQUIRE, "wavefront");
}
DI void set_flag(unsigned char* ws, unsigned off, u64 tag) {
  __hip_atomic_store((GAS u64*)(ws + off), tag, __ATOMIC_RELAXED, __HIP_MEMORY_SCOPE_AGENT);
}
DI v4u ld_sc1(rsrc_t rs, unsigned voff, unsigned soff) { return __builtin_amdgcn_raw_buffer_load_b128(rs, voff, soff, AUX_SC1); }
DI v2u ld_sc1_64(rsrc_t rs, unsigned voff, unsigned soff) { return __builtin_amdgcn_raw_buffer_load_b64(rs, voff, soff, AUX_SC1); }
DI void st_sc1(v4u v, rsrc_t rs, unsigned voff, unsigned soff) { __builtin_amdgcn_raw_buffer_store_b128(v, rs, voff, soff, AUX_SC1); }
DI void st_sc1_64(v2u v, rsrc_t rs, unsigned voff, unsigned soff) { __builtin_amdgcn_raw_buffer_store_b64(v, rs, voff, soff, AUX_SC1); }
DI int wrap(int v, int m) { return v >= m ? v - m : v; }

#define STAMP(k) do { } while (0)
}

__global__ void __launch_bounds__(512, 2)
fused_block(const u16* __restrict__ x, const u16* __restrict__ wqkv, const u16* __restrict__ wo, const u16* __restrict__ w1,
            const u16* __restrict__ b1, const u16* __restrict__ w2, const u16* __restrict__ b2, u16* __restrict__ out,
            unsigned char* ws) {
  __shared__ __attribute__((aligned(1024))) unsigned char smem[LDS_BYTES];
  LAS unsigned char* L = (LAS unsigned char*)smem;
  const unsigned lbase = (unsigned)(size_t)L;
  STAMP(0);
  const int tid = threadIdx.x, l = tid & 63, w = __builtin_amdgcn_readfirstlane(tid >> 6), g = l >> 4, c = l & 15;
  const int b = blockIdx.x, xcd = b & 7, slot = b >> 3;
  const int i1 = 4 * (xcd >> 1) + (slot >> 3), h = 8 * (xcd & 1) + (slot & 7);
  const int u = 4 * xcd + (slot >> 3), ct = slot & 7;
  const bool act24 = ct < 7;
  const int sa = slot >> 3;
  const int r1 = slot % 7, r3 = 2 * ct >= 14 ? 1 : 2 * ct, r4 = (2 * sa + ct) % 7;

  const rsrc_t rX = __builtin_amdgcn_make_buffer_rsrc((void*)x, 0, S_ * D_ * 2, 0x00020000);
  const rsrc_t rQ = __builtin_amdgcn_make_buffer_rsrc((void*)wqkv, 0, D_ * 3 * D_ * 2, 0x00020000);
  const rsrc_t rO = __builtin_amdgcn_make_buffer_rsrc((void*)wo, 0, D_ * D_ * 2, 0x00020000);
  const rsrc_t rA = __builtin_amdgcn_make_buffer_rsrc((void*)w1, 0, D_ * F_ * 2, 0x00020000);
  const rsrc_t rB = __builtin_amdgcn_make_buffer_rsrc((void*)w2, 0, F_ * D_ * 2, 0x00020000);
  const rsrc_t rW = __builtin_amdgcn_make_buffer_rsrc((void*)ws, 0, WS_END, 0x00020000);

  const unsigned vW = (l >> 2) * 2688 + (((l >> 1) & 1) ^ (l >> 5)) * 24 + (l & 1) * 16;
  const unsigned vXa = (128 * i1 + 16 * w + (l >> 3)) * 896 + ((l & 7) ^ g) * 16;
  const unsigned vXb = (vXa ^ 64) + 8 * 896;
  const unsigned sW = (32 * (w >> 2) + 16 * (w & 1)) * 2688 + (1 + ((w >> 1) & 1)) * 896 + 56 * h;
  auto issue_w = [&](int i) { dma16(rQ, vW, sW + wrap(i + r1, 7) * 64 * 2688, lbase + L_KV + w * 1024 + i * 8192); };
  auto issue_xa = [&](int i) { dma16(rX, vXa, wrap(i + r1, 7) * 128, lbase + xslot(i) + w * 2048); };
  auto issue_xb = [&](int i) { dma16(rX, vXb, wrap(i + r1, 7) * 128, lbase + xslot(i) + w * 2048 + 1024); };
  auto issue_q = [&](int m) {
    int n = w + 8 * m;
    if (m == 3) n = n >= 28 ? n - 4 : n;
    dma16(rQ, vW, (64 * wrap((n >> 2) + r1, 7) + 16 * (n & 3)) * 2688 + 56 * h, lbase + L_Q + n * 1024);
  };
  auto stage_issue = [&](int i) {
    issue_w(i);
    issue_xa(i);
    issue_xb(i);
  };
  stage_issue(0);
  unsigned cpf;
  v2u ep;
  {
    u64 pc;
    unsigned sz;
    asm volatile(".Lfb_start:\n\ts_getpc_b64 %0\n\ts_mov_b32 %1, .Lfb_end-.Lfb_start" : "=s"(pc), "=s"(sz));
    const rsrc_t rC = __builtin_amdgcn_make_buffer_rsrc((void*)pc, 0, sz, 0x00020000);
    asm volatile("s_nop 4\n\tbuffer_load_dword %0, %1, %2, 0 offen" : "=v"(cpf) : "v"(w == 7 ? ((slot & 3) * 64 + l) * 128 : 0x7FFFFFF0), "s"(rC) : "memory");
    asm volatile("buffer_load_dwordx2 %0, off, %1, 0 sc1" : "=v"(ep) : "s"(rW) : "memory");
  }
  stage_issue(1);
  STAMP(1);
  const unsigned v8 = (l >> 3) * 896 + (((l >> 1) & 3) ^ g) * 32 + (l & 1) * 16 + ct * 128;
  unsigned v1off;
  {
    const int ch = 64 * (w < 7 ? w : 6) + l, row = (ch * 2341) >> 15, cc = ch - 14 * row, js = cc >> 1;
    const int j = (h & 1) ? (js == 6 ? 0 : js + 1) : js;
    v1off = row * 3584 + (112 * h + 16 * j) * 2 + (cc & 1) * 16;
  }
  const int wc = w < 7 ? w : 6;
  const int mt = w & 3, kh = w >> 2;
  unsigned l8 = (l >> 5) * 1024 + ((g & 1) * 4 + (c >> 2)) * 128 + ((g & 1) * 2 + (c >> 3)) * 32 + (c & 3) * 8;
  const int rk1 = i1 < 14 ? i1 : 13;
  const unsigned lq0 = (l >> 5) * 1024 + (g & 1) * 544 + (c >> 2) * 64 + (c & 3) * 8, lq1 = lq0 ^ 32;
  unsigned lx0 = xslot(0) + w * 2048 + c * 128 + (g ^ (c >> 1)) * 16, lx1 = lx0 ^ 64;
  OPQ(lx0);
  OPQ(lx1);
  const unsigned xw = L_X + (w >> 1) * 1024 + l * 16 + (w & 1) * 8;
  v4u xf[14];
  f32x4 acc[4], acs[4];
  float s1 = 0.f, s2 = 0.f, csp = 0.f;
  const unsigned csa = (lq0 ^ ((w & 1) * 32)) + L_KV + (w >> 2) * 4096 + ((w >> 1) & 1) * 2048;
#pragma unroll
  for (int t = 0; t < 4; ++t) acc[t] = acs[t] = f32x4{0.f, 0.f, 0.f, 0.f};
  const unsigned ONES = 0x3F803F80u;
  const v4u on4 = {ONES, ONES, ONES, ONES};
  u64 tag = 0;
  float rs[4], mn[4];
#pragma unroll
  for (int i = 0; i < 7; ++i) {
    constexpr int NW[7] = {5, 7, 6, 7, 7, 6, 4};
    asm volatile("s_waitcnt vmcnt(%4) lgkmcnt(0)\n\ts_barrier" : "+v"(s1), "+v"(s2), "+v"(ep), "+v"(cpf) : "i"(NW[i]) : "memory");
    if (i == 0) {
      dma16(rA, v1off, rk1 * 32 * 3584, lbase + L_RP + 16 * K + wc * 1024);
      stage_issue(2);
    }
    if (i < 4) issue_w(i + 3);
    if (i == 4 || i == 5) issue_q(2 * (i - 4));
    if (i == 0) STAMP(13);
    if (i == 3) STAMP(16);
    xf[2 * i] = *(LAS v4u*)(L + (i & 3) * 16 * K + lx0);
    xf[2 * i + 1] = *(LAS v4u*)(L + (i & 3) * 16 * K + lx1);
    v4u f[8];
#pragma unroll
    for (int q = 0; q < 8; ++q) f[q] = trfrag(L, ((q & 1) ? lq1 : lq0) + L_KV + (2 * i + (q >> 2)) * 4096 + ((q >> 1) & 1) * 2048, 256);
    v4u fc;
    float cs0[4];
    if (i < 6) fc = trfrag(L, csa + i * 8192, 256);
    if (i == 6) {
#pragma unroll
      for (int t = 0; t < 4; ++t) cs0[t] = *(LAS float*)(L + L_CS1 + t * 64 + c * 4) + *(LAS float*)(L + L_CS1 + (t + 4) * 64 + c * 4);
    }
    SB();
    if (i < 4) issue_xa(i + 3);
    SB();
#pragma unroll
    for (int ss = 0; ss < 2; ++ss)
#pragma unroll
      for (int d = 0; d < 4; ++d) {
        s1 = dot2(xf[2 * i + ss][d], ONES, s1);
        s2 = dot2(xf[2 * i + ss][d], xf[2 * i + ss][d], s2);
      }
    if (i == 6) {
      const f32x4 a1 = rowsum(s1), a2 = rowsum(s2);
#pragma unroll
      for (int r = 0; r < 4; ++r) {
        mn[r] = a1[r] * (-1.0f / 448.0f);
        rs[r] = __builtin_amdgcn_rsqf(fmaxf(__builtin_fmaf(a2[r], 1.0f / 448.0f, -mn[r] * mn[r]), 0.f) + 1e-5f);
      }
    }
#pragma unroll
    for (int q = 0; q < 8; ++q) {
      if (i == 6) {
        if (q < 4) acs[q][0] = cs0[q];
        acs[q & 3] = mfma(on4, f[q], acs[q & 3]);
      }
      acc[q & 3] = mfma(xf[2 * i + (q >> 2)], f[q], acc[q & 3]);
      if (q == 3) {
        SB();
        if (i < 4) issue_xb(i + 3);
        if (i == 4 || i == 5) issue_q(2 * (i - 4) + 1);
        SB();
      }
    }
    if (i < 6) {
#pragma unroll
      for (int d = 0; d < 4; ++d) csp = dot2(fc[d], ONES, csp);
    }
    if (i == 5) {
      csp = colsum(csp);
      if (g == 0) *(LAS float*)(L + L_CS1 + w * 64 + c * 4) = csp;
    }
    SB();
    if (i == 2) {
      const v4u g2 = trfrag(L, ((l >> 5) * 8 + (g & 1) * 4 + (c >> 2)) * 224 + (c & 3) * 8 + L_RP + 16 * K + wc * 32, 3584);
      st_sc1(g2, rW, l * 16, WS_R1 + ((h * 14 + rk1) * 7 + wc) * 1024);
      SB();
      const u64 e = ((u64)(unsigned)__builtin_amdgcn_readfirstlane(ep.y) << 32) | (unsigned)__builtin_amdgcn_readfirstlane(ep.x);
      tag = e * 6364136223846793005ull + 1442695040888963407ull;
    }
  }
  STAMP(2);
#pragma unroll
  for (int t = 0; t < 4; ++t) {
    float v[4];
#pragma unroll
    for (int r = 0; r < 4; ++r) {
      const float d = __builtin_fmaf(mn[r], acs[t][0], acc[t][r]);
      v[r] = t < 2 ? phi(d, rs[r], rs[r] * LOG2E) : d * rs[r];
      if (t == 3) v[r] = c < 4 ? 1.0f : v[r];
      OPQ(v[r]);
    }
    *(LAS v2u*)(L + xw + t * 4096) = v2u{pack2(v[0], v[1]), pack2(v[2], v[3])};
  }
  BAR_VM();
  STAMP(3);
  if (w < 4) {
    f32x4 kv = {0.f, 0.f, 0.f, 0.f};
    v4u a[4], bb[4];
#pragma unroll
    for (int ks = 0; ks < 4; ++ks) {
      a[ks] = *(LAS v4u*)(L + L_X + (((w >> 1) * 4 + ks) * 64 + l) * 16);
      bb[ks] = *(LAS v4u*)(L + L_X + (((2 + (w & 1)) * 4 + ks) * 64 + l) * 16);
    }
#pragma unroll
    for (int ks = 0; ks < 4; ++ks) kv = mfma(a[ks], bb[ks], kv);
    if (w >= 2 && g == 0) kv = f32x4{0.f, 0.f, 0.f, 0.f};
    st_sc1(__builtin_bit_cast(v4u, kv), rW, l * 16, WS_KVP + ((h * 4 + w) * 16 + i1) * 1024);
  }
  v4u pqf;
  {
    constexpr int QB = 8;
    const int rb2 = w < 4 || b >= 136 ? b : b + 256;
    f32x4 aq[2] = {f32x4{0.f, 0.f, 0.f, 0.f}, f32x4{0.f, 0.f, 0.f, 0.f}};
    float csq = 0.f;
    unsigned lqa = lq0 + L_Q, lqb = lq1 + L_Q;
    OPQ(lqa);
    OPQ(lqb);
    const unsigned qa = (lqa ^ ((w & 1) * 32)) + (w >> 1) * 2048;
    v4u fc[4];
#pragma unroll
    for (int m = 0; m < 4; ++m) fc[m] = trfrag(L, qa + (m < 3 ? m * 8192 : (w < 4 ? 3 * 8192 : 0)), 256);
    v4u f[28];
#pragma unroll
    for (int q = 0; q < 6; ++q) f[q] = trfrag(L, ((q & 1) ? lqb : lqa) + (q >> 1) * 2048, 256);
    SB();
    const int ho = (sa & 1) * 4096, wo_ = wrap(w + (sa >> 1) * 3, 7);
    const unsigned on3 = w < 4 ? ONES : 0u;
    float csq2 = 0.f;
#pragma unroll
    for (int d = 0; d < 4; ++d) {
      csq = dot2(fc[0][d], ONES, csq);
      csq2 = dot2(fc[1][d], ONES, csq2);
      csq = dot2(fc[2][d], ONES, csq);
      csq2 = dot2(fc[3][d], on3, csq2);
    }
    csq = colsum(csq + csq2);
    const float mq = colsum(s1) * (-1.0f / 448.0f);
    const float rq = __builtin_amdgcn_rsqf(fmaxf(__builtin_fmaf(colsum(s2), 1.0f / 448.0f, -mq * mq), 0.f) + 1e-5f);
    if (g == 0) *(LAS float*)(L + L_CSQ + w * 64 + c * 4) = csq;
    SB();
#pragma unroll
    for (int q = 0; q < 28; ++q) {
      if (q + 6 < 28) f[q + 6] = trfrag(L, ((q & 1) ? lqb : lqa) + ((q + 6) >> 1) * 2048, 256);
      aq[q & 1] = mfma(f[q], xf[q >> 1], aq[q & 1]);
      if (q == QB) {
        asm volatile("s_waitcnt vmcnt(0)\n\ts_barrier" ::: "memory");
        if (tid == 0) set_flag(ws, WS_F1 + (h * 16 + i1) * FS, tag);
        STAMP(4);
        dma16(rB, v8 - ct * 128, (32 * (rb2 % 56) + 8 * (w & 3)) * 896 + (rb2 / 56) * 128, lbase + w * 1024);
      }
      if (q == QB + 2 && w < 7) dma16x4<8 * 896>(rO, v8, wo_ * 64 * 896 + ho * 7, lbase + L_WO + wo_ * 8192 + ho);
      if (q == QB + 10 && w < 7) dma16x4<8 * 896>(rO, v8, wo_ * 64 * 896 + (4096 - ho) * 7, lbase + L_WO + wo_ * 8192 + 4096 - ho);
      SB();
    }
    if (w < 7) asm volatile("s_waitcnt vmcnt(8) lgkmcnt(0)\n\ts_barrier" ::: "memory");
    else BAR_VM();
    st_sc1(trfrag(L, (l8 ^ ((w & 3) * 32)) + (w >> 2) * 4096, 2048), rW, l * 16, WS_R2 + (rb2 * 4 + (w & 3)) * 1024);
#pragma unroll
    for (int t = 0; t < 2; ++t) {
      f32x4 cs = *(LAS f32x4*)(L + L_CSQ + t * 64 + g * 16);
#pragma unroll
      for (int m = 1; m < 4; ++m) cs += *(LAS f32x4*)(L + L_CSQ + (t + 2 * m) * 64 + g * 16);
      float v[4];
#pragma unroll
      for (int r = 0; r < 4; ++r) v[r] = phi(__builtin_fmaf(mq, cs[r], aq[t][r]), rq, rq * LOG2E);
      pqf[2 * t] = pack2(v[0], v[1]);
      pqf[2 * t + 1] = pack2(v[2], v[3]);
    }
    asm volatile("" : "+v"(pqf));
  }
  unsigned kvo[4];
#pragma unroll
  for (int n = 0; n < 4; ++n) {
    kvo[n] = WS_KVP + ((h * 4 + (w & 3)) * 16 + 8 * (w >> 2) + 2 * ((n + sa) & 3)) * 1024;
    asm volatile("" : "+s"(kvo[n]));
  }
  STAMP(5);
  if (w == 7) poll_flags(ws, WS_F1 + h * 16 * FS, c, tag);
  BAR();
  STAMP(6);
  {
    v4u p[8];
#pragma unroll
    for (int n = 0; n < 8; ++n) {
      p[n] = ld_sc1(rW, l * 16 + (n & 1) * 1024, kvo[n >> 1]);
      SB();
    }
    SB();
    f32x4 sum = __builtin_bit_cast(f32x4, p[0]);
#pragma unroll
    for (int n = 1; n < 8; ++n) sum += __builtin_bit_cast(f32x4, p[n]);
    *(LAS f32x4*)(L + L_KVS + w * 1024 + l * 16) = sum;
  }
  BAR();
  STAMP(7);
  {
    f32x4 T[4], Z[2];
#pragma unroll
    for (int dt = 0; dt < 2; ++dt)
      Z[dt] = *(LAS f32x4*)(L + L_KVS + (dt * 2 + 1) * 1024 + g * 256) + *(LAS f32x4*)(L + L_KVS + (dt * 2 + 5) * 1024 + g * 256);
#pragma unroll
    for (int t = 0; t < 4; ++t) T[t] = *(LAS f32x4*)(L + L_KVS + t * 1024 + l * 16) + *(LAS f32x4*)(L + L_KVS + (t + 4) * 1024 + l * 16);
    v4u f0 = {pack2(T[0].x, T[0].y), pack2(T[0].z, T[0].w), pack2(T[2].x, T[2].y), pack2(T[2].z, T[2].w)};
    v4u f1 = {pack2(T[1].x, T[1].y), pack2(T[1].z, T[1].w), pack2(T[3].x, T[3].y), pack2(T[3].z, T[3].w)};
    v4u fz = {pack2(Z[0].x, Z[0].y), pack2(Z[0].z, Z[0].w), pack2(Z[1].x, Z[1].y), pack2(Z[1].z, Z[1].w)};
    const f32x4 z4 = {0.f, 0.f, 0.f, 0.f};
    f32x4 den = mfma(fz, pqf, z4), n0 = mfma(f0, pqf, z4), n1 = mfma(f1, pqf, z4);
    const float rd = __builtin_amdgcn_rcpf(den.x + 1e-6f);
    const float rd1 = g == 0 ? 0.f : rd;
    v4u of = {pack2(n0.x * rd, n0.y * rd), pack2(n0.z * rd, n0.w * rd), pack2(n1.x * rd1, n1.y * rd1), pack2(n1.z * rd1, n1.w * rd1)};
    st_sc1(of, rW, l * 16, WS_O + ((8 * i1 + w) * 16 + h) * 1024);
  }
  BAR_VM();
  if (tid == 0) {
    set_flag(ws, WS_F2 + (i1 * 16 + h) * FS, tag);
    set_flag(ws, WS_FR + b * 8, tag);
  }
  STAMP(8);
  v2u bb1 = *(const GAS v2u*)(b1 + 112 * h + 16 * ((h & 1) ? (wc == 6 ? 0 : wc + 1) : wc) + 4 * g);
  if (w < 7) {
#pragma unroll
    for (int s = 0; s < 14; ++s) {
      const int k = wrap(s + 4 * sa, 14);
      dma16s(rW, l * 16, WS_R1 + ((h * 14 + k) * 7 + w) * 1024, lbase + L_W1 + wrap(k + 14 - r3, 14) * 7168 + w * 1024);
    }
  }
  v4u hres;
  if (act24) {
    unsigned wb[4];
#pragma unroll
    for (int j = 0; j < 4; ++j) wb[j] = (l8 ^ (kh * 64) ^ (j * 32)) + L_WO + kh * (224 * 128);
    auto wfrag = [&](int q) {
      const int n = q >> 2, jl = q & 3;
      const unsigned po = (((n >> 1) + ct) & 3) * (56 * 128) + (n & 1) * (28 * 128);
      return trfrag2(L, wb[jl ^ ((n & 1) * 2)] + po, wb[jl ^ (((n & 1) ^ 1) * 2)] + po + 12 * 128);
    };
    unsigned oo[4];
#pragma unroll
    for (int n = 0; n < 4; ++n) {
      oo[n] = WS_O + ((4 * u + mt) * 16 + 8 * kh + 2 * ((n + ct) & 3)) * 1024;
      asm volatile("" : "+s"(oo[n]));
    }
    v4u wf[32];
#pragma unroll
    for (int q = 0; q < 32; ++q) wf[q] = wfrag(q);
    const int m2 = 4 * u + mt;
    if (w == 7) poll_flags(ws, WS_F2 + (u >> 1) * 16 * FS, c, tag);
    BAR();
    STAMP(10);
    v4u of[8];
    auto oload = [&](int n) { of[n] = ld_sc1(rW, l * 16 + (n & 1) * 1024, oo[n >> 1]); };
#pragma unroll
    for (int n = 0; n < 8; ++n) {
      oload(n);
      SB();
    }
    const unsigned vxr = (64 * u + 16 * mt + c) * 896 + (64 * ct + 32 * kh + 4 * g) * 2;
    v2u xr0, xr1;
    f32x4 a2[4];
#pragma unroll
    for (int j = 0; j < 4; ++j) a2[j] = f32x4{0.f, 0.f, 0.f, 0.f};
#pragma unroll
    for (int q = 0; q < 32; ++q) {
      if (q == 4) xr0 = __builtin_amdgcn_raw_buffer_load_b64(rX, vxr, 0, 0);
      if (q == 5) xr1 = __builtin_amdgcn_raw_buffer_load_b64(rX, vxr + 32, 0, 0);
      a2[q & 3] = mfma(wf[q], of[q >> 2], a2[q & 3]);
      SB();
    }
    *(LAS f32x4*)(L + L_EX + w * 2048 + l * 16) = a2[2];
    *(LAS f32x4*)(L + L_EX + w * 2048 + 1024 + l * 16) = a2[3];
    a2[0] += f32x4{bflo(xr0.x), bfhi(xr0.x), bflo(xr0.y), bfhi(xr0.y)};
    a2[1] += f32x4{bflo(xr1.x), bfhi(xr1.x), bflo(xr1.y), bfhi(xr1.y)};
    BAR();
    STAMP(11);
    a2[0] += *(LAS f32x4*)(L + L_EX + (w ^ 4) * 2048 + l * 16);
    a2[1] += *(LAS f32x4*)(L + L_EX + (w ^ 4) * 2048 + 1024 + l * 16);
    hres = v4u{pack2(a2[0].x, a2[0].y), pack2(a2[0].z, a2[0].w), pack2(a2[1].x, a2[1].y), pack2(a2[1].z, a2[1].w)};
    st_sc1(hres, rW, l * 16, WS_H + (m2 * 16 + 2 * ct + kh) * 1024);
    float t1 = 0.f, t2 = 0.f;
#pragma unroll
    for (int d = 0; d < 4; ++d) {
      t1 = dot2(hres[d], ONES, t1);
      t2 = dot2(hres[d], hres[d], t2);
    }
    t1 = colsum(t1);
    t2 = colsum(t2);
    if (g == 0) st_sc1_64(v2u{asu(t1), asu(t2)}, rW, c * 8, WS_ST + (m2 * 16 + 2 * ct + kh) * 128);
    if (w == 6) {
#pragma unroll 1
      for (unsigned it = 0; it < SPIN_CAP; ++it) {
        const v4u fa = ld_sc1(rW, l * 32, WS_FR), fb = ld_sc1(rW, l * 32 + 16, WS_FR);
        const unsigned tl = (unsigned)tag, th = (unsigned)(tag >> 32);
        if (__all(fa.x == tl && fa.y == th && fa.z == tl && fa.w == th && fb.x == tl && fb.y == th && fb.z == tl && fb.w == th)) break;
        asm volatile("s_sleep 1" ::: "memory");
      }
    }
    BAR_VM();
    if (tid == 0) {
      set_flag(ws, WS_F3 + (u * 8 + ct) * FS, tag);
      if (b == 0) set_flag(ws, WS_EPOCH, tag);
    }
  } else {
    BAR_VM();
  }
  STAMP(12);
  v4u wf2[7][4];
  unsigned w2o[7];
#pragma unroll
  for (int kk = 0; kk < 7; ++kk) {
    w2o[kk] = WS_R2 + (ct * 56 + 7 * w + wrap(kk + r4, 7)) * 4096;
    asm volatile("" : "+s"(w2o[kk]));
  }
  auto w2load = [&](int i) { wf2[i >> 2][i & 3] = ld_sc1(rW, l * 16 + (i & 3) * 1024, w2o[i >> 2]); };
  asm volatile("" : "+v"(bb1));
  unsigned lP3 = L_W1 + l * 16, lP3b = lP3 + 7 * 7168;
  OPQ(lP3b);
  {
    v4u f[14];
#pragma unroll
    for (int s = 0; s < 14; ++s) f[s] = *(LAS v4u*)(L + (s < 7 ? lP3 + s * 7168 : lP3b + (s - 7) * 7168) + wc * 1024);
    SB();
    f32x4 cs = {0.f, 0.f, 0.f, 0.f};
#pragma unroll
    for (int s = 0; s < 14; ++s) {
      cs = mfma(f[s], on4, cs);
      if (s < W2_A) w2load(s);
      SB();
    }
    if (c == 0) {
      *(LAS f32x4*)(L + L_C1 + wc * 64 + g * 16) = cs * GC;
      *(LAS f32x4*)(L + L_C1 + 448 + wc * 64 + g * 16) = f32x4{bflo(bb1.x), bfhi(bb1.x), bflo(bb1.y), bfhi(bb1.y)} * GC;
    }
  }
  STAMP(9);
  const int m3 = 8 * i1 + w;
  {
    auto fs = [](int q) { return q < 56 ? q >> 2 : (q - 56) % 14; };
    auto fj = [](int q) { return q < 56 ? q & 3 : 4 + (q - 56) / 14; };
    auto frag = [&](int q) {
      const int s = fs(q), j = fj(q);
      return *(LAS v4u*)(L + (s < 7 ? lP3 + s * 7168 : lP3b + (s - 7) * 7168) + j * 1024);
    };
    constexpr int PF3 = 6;
    v4u f[98];
#pragma unroll
    for (int q = 0; q < PF3; ++q) f[q] = frag(q);
    unsigned ho[14];
#pragma unroll
    for (int s = 0; s < 14; ++s) {
      ho[s] = WS_H + (m3 * 16 + wrap(s + r3, 14)) * 1024;
      asm volatile("" : "+s"(ho[s]));
    }
    if (w == 7) poll_flags(ws, WS_F3 + i1 * 16 * FS, (c & 7) == 7 ? 0 : c, tag);
    BAR();
    STAMP(14);
    v4u hf[14];
    auto hld = [&](int s) { hf[s] = ld_sc1(rW, l * 16, ho[s]); };
    constexpr int HA = 14;
#pragma unroll
    for (int s = 0; s < HA; ++s) {
      hld(s);
      SB();
    }
    v2u st[4];
    f32x4 a3[7];
#pragma unroll
    for (int j = 0; j < 7; ++j) a3[j] = f32x4{0.f, 0.f, 0.f, 0.f};
#pragma unroll
    for (int q = 0; q < 56; ++q) {
      f[q + PF3] = frag(q + PF3);
      a3[fj(q)] = mfma(f[q], hf[fs(q)], a3[fj(q)]);
      if (q >= 8 && q < 12) st[q - 8] = ld_sc1_64(rW, c * 8 + g * 128 + (q - 8) * 512, WS_ST + m3 * 2048);
      if (q >= 44 && q % 4 == 0) w2load(W2_A + (q - 44) / 4);
      SB();
    }
    STAMP(20);
    float t1 = asf(st[0].x) + asf(st[1].x) + asf(st[2].x) + (g < 2 ? asf(st[3].x) : 0.f);
    float t2 = asf(st[0].y) + asf(st[1].y) + asf(st[2].y) + (g < 2 ? asf(st[3].y) : 0.f);
    const float mean2 = colsum(t1) * (1.0f / 448.0f);
    const float rs2 = __builtin_amdgcn_rsqf(fmaxf(__builtin_fmaf(colsum(t2), 1.0f / 448.0f, -mean2 * mean2), 0.f) + 1e-5f);
    const float nm2 = -mean2 * rs2, rs2c = rs2 * GC;
    const unsigned ksp = (7 * h + 1) >> 1;
    const unsigned ob = WS_H1 + (m3 * 56 + ksp) * 1024;
    unsigned pk[14];
    auto epi1 = [&](int j, int r, const f32x4& cs, const f32x4& bb) {
      return gelu_s(__builtin_fmaf(a3[j][r], rs2c, __builtin_fmaf(nm2, cs[r], bb[r])));
    };
#pragma unroll
    for (int jj = 0; jj < 7; ++jj) {
      f32x4 cs = *(LAS f32x4*)(L + L_C1 + jj * 64 + g * 16);
      f32x4 bb = *(LAS f32x4*)(L + L_C1 + 448 + jj * 64 + g * 16);
      SB();
      float y[4];
#pragma unroll
      for (int s = 0; s < 7; ++s) {
        const int q = 56 + jj * 7 + s;
        if (jj < 6) {
          if (q + PF3 < 98) f[q + PF3] = frag(q + PF3);
          a3[fj(q)] = mfma(f[q], hf[fs(q)], a3[fj(q)]);
          if ((s == 0 || s == 3 || s == 5) && 3 + jj * 3 + (s + 1) / 3 < W2_B) w2load(W2_A + 3 + jj * 3 + (s + 1) / 3);
        }
        if (s == 2 || s == 4) {
          y[s - 2] = epi1(jj, s - 2, cs, bb);
          y[s - 1] = epi1(jj, s - 1, cs, bb);
        }
        if (s == 3 || s == 5) pk[2 * jj + (s - 3) / 2] = pack2(y[s - 3], y[s - 2]);
        if (s != 2 && s != 4) SB();
      }
      if (jj & 1) st_sc1(v4u{pk[2 * jj - 2], pk[2 * jj - 1], pk[2 * jj], pk[2 * jj + 1]}, rW, l * 16 + (jj >> 1) * 1024, ob);
    }
    st_sc1_64(v2u{pk[12], pk[13]}, rW, l * 16, (h & 1) ? ob - 1016 : ob + 3072);
    STAMP(21);
    BAR_VM();
    if (tid == 0) set_flag(ws, WS_F4 + (i1 * 16 + h) * FS, tag);
  }
  STAMP(15);
  if (act24) {
#pragma unroll
    for (int i = W2_A + W2_B; i < 28; ++i) w2load(i);
    v4u bb2 = *(const GAS v4u*)(b2 + 64 * ct + (tid & 7) * 8);
    *(LAS v2u*)(L + L_HT + (16 * mt + c) * 144 + (32 * kh + 4 * g) * 2) = v2u{hres.x, hres.y};
    *(LAS v2u*)(L + L_HT + (16 * mt + c) * 144 + (32 * kh + 16 + 4 * g) * 2) = v2u{hres.z, hres.w};
    unsigned ko[7], mo[4];
#pragma unroll
    for (int kk = 0; kk < 7; ++kk) {
      ko[kk] = l * 16 + wrap(kk + r4, 7) * 1024;
      asm volatile("" : "+v"(ko[kk]));
    }
#pragma unroll
    for (int m = 0; m < 4; ++m) {
      mo[m] = WS_H1 + ((4 * u + ((m + ct) & 3)) * 56 + 7 * w) * 1024;
      asm volatile("" : "+s"(mo[m]));
    }
    if (w == 7) poll_flags(ws, WS_F4 + (u >> 1) * 16 * FS, c, tag);
    BAR();
    STAMP(17);
    v4u hf[28];
    auto hload = [&](int i) {
      const int m = i / 7, kk = i % 7;
      hf[i] = ld_sc1(rW, ko[kk], mo[m]);
    };
    constexpr int HB = 10;
#pragma unroll
    for (int i = 0; i < HB; ++i) {
      hload(i);
      SB();
    }
#pragma unroll
    for (int m = 0; m < 4; ++m) {
      f32x4 a4[4];
#pragma unroll
      for (int j = 0; j < 4; ++j) a4[j] = f32x4{0.f, 0.f, 0.f, 0.f};
#pragma unroll
      for (int kk = 0; kk < 7; ++kk) {
        if (7 * m + kk + HB < 28) hload(7 * m + kk + HB);
#pragma unroll
        for (int j = 0; j < 4; ++j) a4[j] = mfma(wf2[kk][j], hf[7 * m + kk], a4[j]);
        SB();
      }
      const unsigned pw = L_PT + w * 17408 + ((m + ct) & 3) * (16 * 272) + c * 272 + g * 16;
#pragma unroll
      for (int j = 0; j < 4; ++j) *(LAS f32x4*)(L + pw + j * 64) = a4[j];
      SB();
    }
    BAR();
    STAMP(18);
    const unsigned pa = L_PT + (tid >> 3) * 272 + (tid & 7) * 32;
    f32x4 o0 = *(LAS f32x4*)(L + pa), o1 = *(LAS f32x4*)(L + pa + 16);
#pragma unroll
    for (int ww = 1; ww < 8; ++ww) {
      o0 += *(LAS f32x4*)(L + pa + ww * 17408);
      o1 += *(LAS f32x4*)(L + pa + ww * 17408 + 16);
    }
    v4u hh = *(LAS v4u*)(L + L_HT + (tid >> 3) * 144 + (tid & 7) * 16);
    constexpr float IG = 1.0f / GC;
    v4u ov = {pack2(o0.x * IG + (bflo(bb2.x) + bflo(hh.x)), o0.y * IG + (bfhi(bb2.x) + bfhi(hh.x))),
              pack2(o0.z * IG + (bflo(bb2.y) + bflo(hh.y)), o0.w * IG + (bfhi(bb2.y) + bfhi(hh.y))),
              pack2(o1.x * IG + (bflo(bb2.z) + bflo(hh.z)), o1.y * IG + (bfhi(bb2.z) + bfhi(hh.z))),
              pack2(o1.z * IG + (bflo(bb2.w) + bflo(hh.w)), o1.w * IG + (bfhi(bb2.w) + bfhi(hh.w)))};
    *(GAS v4u*)(out + (64 * u + (tid >> 3)) * 448 + 64 * ct + (tid & 7) * 8) = ov;
  }
  STAMP(19);
  asm volatile(".Lfb_end:" ::: "memory");
}

extern "C" void kernel_launch(void* const* d_in, const int* in_sizes, int n_in, void* d_out, int out_size, void* d_ws,
                              size_t ws_size, hipStream_t stream) {
  (void)in_sizes; (void)n_in; (void)out_size; (void)ws_size;
  fused_block<<<256, 512, 0, stream>>>((const u16*)d_in[0], (const u16*)d_in[1], (const u16*)d_in[2], (const u16*)d_in[4],
                                       (const u16*)d_in[5], (const u16*)d_in[6], (const u16*)d_in[7], (u16*)d_out,
                                       (unsigned char*)d_ws);
}
```

```cpp
#include <hip/hip_runtime.h>
#include <hip/hip_bf16.h>

#define LAS __attribute__((address_space(3)))
#define GAS __attribute__((address_space(1)))
#define DI __device__ __forceinline__
typedef unsigned v4u __attribute__((ext_vector_type(4)));
typedef unsigned v2u __attribute__((ext_vector_type(2)));
typedef float f32x4 __attribute__((ext_vector_type(4)));
typedef float f32x2 __attribute__((ext_vector_type(2)));
typedef short s16x4 __attribute__((ext_vector_type(4)));
typedef __bf16 bf16x8 __attribute__((ext_vector_type(8)));
typedef __bf16 bf16x2 __attribute__((ext_vector_type(2)));
typedef unsigned long long u64;
typedef unsigned short u16;
typedef __amdgpu_buffer_rsrc_t rsrc_t;

#define W2_A 6
#define W2_B 16

namespace {
constexpr int S_ = 2048, D_ = 448, F_ = 1792;
constexpr unsigned FS = 64;
constexpr unsigned WS_EPOCH = 0;
constexpr unsigned WS_F1 = 4096;
constexpr unsigned WS_F2 = WS_F1 + 256 * FS;
constexpr unsigned WS_F3 = WS_F2 + 256 * FS;
constexpr unsigned WS_F4 = WS_F3 + 256 * FS;
constexpr unsigned WS_FR = WS_F4 + 256 * FS;
constexpr unsigned WS_KVP = 131072;
constexpr unsigned WS_O = WS_KVP + (1u << 20);
constexpr unsigned WS_H = WS_O + (2u << 20);
constexpr unsigned WS_ST = WS_H + (2u << 20);
constexpr unsigned WS_H1 = WS_ST + (256u << 10);
constexpr unsigned WS_R1 = WS_H1 + 128u * 56u * 1024u;
constexpr unsigned WS_R2 = WS_R1 + 16u * 14u * 7168u;
constexpr unsigned WS_END = WS_R2 + 7u * 56u * 4096u;
constexpr unsigned WS_DBG = 32u << 20;
constexpr unsigned K = 1024;
constexpr unsigned L_KV = 0, L_Q = 56 * K, L_X = 84 * K, L_WO = 100 * K, L_SC = 156 * K;
constexpr unsigned L_CSQ = L_SC, L_CS1 = L_SC + 512, L_C1 = L_SC + 1024, L_CNT = L_SC + 2048;
constexpr unsigned L_EX = 104 * K, L_KVS = 88 * K, L_RP = 0;
constexpr unsigned L_W1 = 0, L_PT = 0, L_HT = 140 * K;
constexpr unsigned LDS_BYTES = 163840;
constexpr int AUX_SC1 = 16;
constexpr unsigned SPIN_CAP = 1u << 18;
constexpr unsigned xslot(int i) { return 88 * K + (i & 3) * 16 * K; }

DI f32x4 mfma(v4u a, v4u b, f32x4 c) {
  return __builtin_amdgcn_mfma_f32_16x16x32_bf16(__builtin_bit_cast(bf16x8, a), __builtin_bit_cast(bf16x8, b), c, 0, 0, 0);
}
DI f32x4 rowsum(float v) {
  asm volatile("s_nop 2" : "+v"(v));
  return __builtin_amdgcn_mfma_f32_16x16x4f32(v, 1.0f, f32x4{0.f, 0.f, 0.f, 0.f}, 0, 0, 0);
}
DI float colsum(float v) {
  asm volatile("s_nop 2" : "+v"(v));
  return __builtin_amdgcn_mfma_f32_16x16x4f32(1.0f, v, f32x4{0.f, 0.f, 0.f, 0.f}, 0, 0, 0)[0];
}
DI unsigned pack2(float a, float b) {
  bf16x2 v = {(__bf16)a, (__bf16)b};
  return __builtin_bit_cast(unsigned, v);
}
DI float asf(unsigned u) { return __builtin_bit_cast(float, u); }
DI unsigned asu(float f) { return __builtin_bit_cast(unsigned, f); }
DI float bflo(unsigned u) { return asf(u << 16); }
DI float bfhi(unsigned u) { return asf(u & 0xffff0000u); }
DI float dot2(unsigned a, unsigned b, float c) {
  return __builtin_amdgcn_fdot2_f32_bf16(__builtin_bit_cast(bf16x2, a), __builtin_bit_cast(bf16x2, b), c, false);
}
DI v4u trfrag2(LAS unsigned char* L, unsigned a, unsigned b) {
  s16x4 lo = __builtin_amdgcn_ds_read_tr16_b64_v4i16((LAS s16x4*)(L + a));
  s16x4 hi = __builtin_amdgcn_ds_read_tr16_b64_v4i16((LAS s16x4*)(L + b));
  v2u l2 = __builtin_bit_cast(v2u, lo), h2 = __builtin_bit_cast(v2u, hi);
  return v4u{l2.x, l2.y, h2.x, h2.y};
}
DI v4u trfrag(LAS unsigned char* L, unsigned a, unsigned dt) { return trfrag2(L, a, a + dt); }
DI void dma16(rsrc_t rs, unsigned voff, unsigned soff, unsigned ldst) {
  asm volatile("s_mov_b32 m0, %0\n\ts_nop 0\n\tbuffer_load_dwordx4 %1, %2, %3 offen lds" ::"s"(ldst), "v"(voff), "s"(rs), "s"(soff) : "memory");
}
DI void dma16s(rsrc_t rs, unsigned voff, unsigned soff, unsigned ldst) {
  asm volatile("s_mov_b32 m0, %0\n\ts_nop 0\n\tbuffer_load_dwordx4 %1, %2, %3 offen sc1 lds" ::"s"(ldst), "v"(voff), "s"(rs), "s"(soff) : "memory");
}
DI void dma16x2(rsrc_t rs, unsigned voff, unsigned voff2, unsigned soff, unsigned ldst) {
  asm volatile("s_mov_b32 m0, %0\n\ts_nop 0\n\tbuffer_load_dwordx4 %1, %3, %4 offen lds\n\tbuffer_load_dwordx4 %2, %3, %4 offen offset:1024 lds"
               ::"s"(ldst), "v"(voff), "v"(voff2), "s"(rs), "s"(soff) : "memory");
}
template <int STR> DI void dma16x4(rsrc_t rs, unsigned voff, unsigned soff, unsigned ldst) {
  unsigned t;
  asm volatile("s_mov_b32 m0, %1\n\ts_add_u32 %0, %4, %5\n\tbuffer_load_dwordx4 %2, %3, %4 offen lds\n\t"
               "buffer_load_dwordx4 %2, %3, %0 offen offset:1024 lds\n\ts_add_u32 %0, %0, %5\n\t"
               "buffer_load_dwordx4 %2, %3, %0 offen offset:2048 lds\n\ts_add_u32 %0, %0, %5\n\t"
               "buffer_load_dwordx4 %2, %3, %0 offen offset:3072 lds"
               : "=&s"(t) : "s"(ldst), "v"(voff), "s"(rs), "s"(soff), "i"(STR - 1024) : "memory", "scc");
}
#define BAR() asm volatile("s_waitcnt lgkmcnt(0)\n\ts_barrier" ::: "memory")
#define BAR_VM() asm volatile("s_waitcnt vmcnt(0) lgkmcnt(0)\n\ts_barrier" ::: "memory")
#define DRAIN() asm volatile("s_waitcnt vmcnt(0)" ::: "memory")
#define SB() __builtin_amdgcn_sched_barrier(0)
#define OPQ(x) asm("" : "+v"(x))
constexpr float LOG2E = 1.4426950408889634f;
DI float phi(float t, float rs, float rl) { return __builtin_amdgcn_exp2f(fminf(t * rl, 0.f)) + fmaxf(__builtin_fmaf(t, rs, 1e-6f), 1e-6f); }
constexpr float GC = 0.4686686930532063f, GK = -4.91222953926424f;
DI float gelu_s(float v) {
  float e = __builtin_amdgcn_exp2f(v * __builtin_fmaf(-v, v, GK));
  float y = v * __builtin_amdgcn_rcpf(1.0f + e);
  OPQ(y);
  return y;
}
DI void poll_flags(unsigned char* ws, unsigned off, int idx, u64 tag) {
  const GAS u64* q = (const GAS u64*)(ws + off + idx * FS);
#pragma unroll 1
  for (unsigned it = 0; it < SPIN_CAP; ++it) {
    u64 v = __hip_atomic_load(q, __ATOMIC_RELAXED, __HIP_MEMORY_SCOPE_AGENT);
    if (__all(v == tag)) break;
    __builtin_amdgcn_s_sleep(1);
  }
  __builtin_amdgcn_fence(__ATOMIC_ACQUIRE, "wavefront");
}
DI void set_flag(unsigned char* ws, unsigned off, u64 tag) {
  __hip_atomic_store((GAS u64*)(ws + off), tag, __ATOMIC_RELAXED, __HIP_MEMORY_SCOPE_AGENT);
}
DI v4u ld_sc1(rsrc_t rs, unsigned voff, unsigned soff) { return __builtin_amdgcn_raw_buffer_load_b128(rs, voff, soff, AUX_SC1); }
DI v2u ld_sc1_64(rsrc_t rs, unsigned voff, unsigned soff) { return __builtin_amdgcn_raw_buffer_load_b64(rs, voff, soff, AUX_SC1); }
DI void st_sc1(v4u v, rsrc_t rs, unsigned voff, unsigned soff) { __builtin_amdgcn_raw_buffer_store_b128(v, rs, voff, soff, AUX_SC1); }
DI void st_sc1_64(v2u v, rsrc_t rs, unsigned voff, unsigned soff) { __builtin_amdgcn_raw_buffer_store_b64(v, rs, voff, soff, AUX_SC1); }
DI int wrap(int v, int m) { return v >= m ? v - m : v; }

#define STAMP(k) do { } while (0)
}

__global__ void __launch_bounds__(512, 2)
fused_block(const u16* __restrict__ x, const u16* __restrict__ wqkv, const u16* __restrict__ wo, const u16* __restrict__ w1,
            const u16* __restrict__ b1, const u16* __restrict__ w2, const u16* __restrict__ b2, u16* __restrict__ out,
            unsigned char* ws) {
  __shared__ __attribute__((aligned(1024))) unsigned char smem[LDS_BYTES];
  LAS unsigned char* L = (LAS unsigned char*)smem;
  const unsigned lbase = (unsigned)(size_t)L;
  STAMP(0);
  const int tid = threadIdx.x, l = tid & 63, w = __builtin_amdgcn_readfirstlane(tid >> 6), g = l >> 4, c = l & 15;
  const int b = blockIdx.x, xcd = b & 7, slot = b >> 3;
  const int i1 = 4 * (xcd >> 1) + (slot >> 3), h = 8 * (xcd & 1) + (slot & 7);
  const int u = 4 * xcd + (slot >> 3), ct = slot & 7;
  const bool act24 = ct < 7;
  const int sa = slot >> 3;
  const int r1 = slot % 7, r3 = (4 * sa + 2 * ct) % 14, r4 = (2 * sa + ct) % 7;

  const rsrc_t rX = __builtin_amdgcn_make_buffer_rsrc((void*)x, 0, S_ * D_ * 2, 0x00020000);
  const rsrc_t rQ = __builtin_amdgcn_make_buffer_rsrc((void*)wqkv, 0, D_ * 3 * D_ * 2, 0x00020000);
  const rsrc_t rO = __builtin_amdgcn_make_buffer_rsrc((void*)wo, 0, D_ * D_ * 2, 0x00020000);
  const rsrc_t rA = __builtin_amdgcn_make_buffer_rsrc((void*)w1, 0, D_ * F_ * 2, 0x00020000);
  const rsrc_t rB = __builtin_amdgcn_make_buffer_rsrc((void*)w2, 0, F_ * D_ * 2, 0x00020000);
  const rsrc_t rW = __builtin_amdgcn_make_buffer_rsrc((void*)ws, 0, WS_END, 0x00020000);

  const unsigned vW = (l >> 2) * 2688 + (((l >> 1) & 1) ^ (l >> 5)) * 24 + (l & 1) * 16;
  const unsigned vXa = (128 * i1 + 16 * w + (l >> 3)) * 896 + ((l & 7) ^ g) * 16;
  const unsigned vXb = (vXa ^ 64) + 8 * 896;
  const unsigned sW = (32 * (w >> 2) + 16 * (w & 1)) * 2688 + (1 + ((w >> 1) & 1)) * 896 + 56 * h;
  auto issue_w = [&](int i) { dma16(rQ, vW, sW + wrap(i + r1, 7) * 64 * 2688, lbase + L_KV + w * 1024 + i * 8192); };
  auto issue_xa = [&](int i) { dma16(rX, vXa, wrap(i + r1, 7) * 128, lbase + xslot(i) + w * 2048); };
  auto issue_xb = [&](int i) { dma16(rX, vXb, wrap(i + r1, 7) * 128, lbase + xslot(i) + w * 2048 + 1024); };
  auto issue_q = [&](int m) {
    int n = w + 8 * m;
    if (m == 3) n = n >= 28 ? n - 4 : n;
    dma16(rQ, vW, (64 * wrap((n >> 2) + r1, 7) + 16 * (n & 3)) * 2688 + 56 * h, lbase + L_Q + n * 1024);
  };
  auto stage_issue = [&](int i) {
    issue_w(i);
    issue_xa(i);
    issue_xb(i);
  };
  stage_issue(0);
  const unsigned v8 = (l >> 3) * 896 + (((l >> 1) & 3) ^ g) * 32 + (l & 1) * 16 + ct * 128;
  unsigned v1off;
  {
    const int ch = 64 * (w < 7 ? w : 6) + l, row = (ch * 2341) >> 15, cc = ch - 14 * row, js = cc >> 1;
    const int j = (h & 1) ? (js == 6 ? 0 : js + 1) : js;
    v1off = row * 3584 + (112 * h + 16 * j) * 2 + (cc & 1) * 16;
  }
  const int wc = w < 7 ? w : 6;
  const int mt = w & 3, kh = w >> 2;
  unsigned l8 = (l >> 5) * 1024 + ((g & 1) * 4 + (c >> 2)) * 128 + ((g & 1) * 2 + (c >> 3)) * 32 + (c & 3) * 8;
  const int rk1 = i1 < 14 ? i1 : 13, rb2 = w < 4 || b >= 136 ? b : b + 256;
  v4u r2f;
  unsigned cpf;
  v2u ep;
  {
    u64 pc;
    unsigned sz;
    asm volatile(".Lfb_start:\n\ts_getpc_b64 %0\n\ts_mov_b32 %1, .Lfb_end-.Lfb_start" : "=s"(pc), "=s"(sz));
    const rsrc_t rC = __builtin_amdgcn_make_buffer_rsrc((void*)pc, 0, sz, 0x00020000);
    asm volatile("s_nop 4\n\tbuffer_load_dword %0, %1, %2, 0 offen" : "=v"(cpf) : "v"(w == 7 ? ((slot & 3) * 64 + l) * 128 : 0x7FFFFFF0), "s"(rC) : "memory");
    asm volatile("buffer_load_dwordx2 %0, off, %1, 0 sc1" : "=v"(ep) : "s"(rW) : "memory");
  }
  STAMP(1);
  const unsigned lq0 = (l >> 5) * 1024 + (g & 1) * 544 + (c >> 2) * 64 + (c & 3) * 8, lq1 = lq0 ^ 32;
  unsigned lx0 = xslot(0) + w * 2048 + c * 128 + (g ^ (c >> 1)) * 16, lx1 = lx0 ^ 64;
  OPQ(lx0);
  OPQ(lx1);
  const unsigned xw = L_X + (w >> 1) * 1024 + l * 16 + (w & 1) * 8;
  v4u xf[14];
  f32x4 acc[4], as1 = {0.f, 0.f, 0.f, 0.f}, as2 = as1, acs = as1;
  const unsigned csa = (lq0 ^ ((w & 1) * 32)) + L_KV + (w >> 2) * 4096 + ((w >> 1) & 1) * 2048;
#pragma unroll
  for (int t = 0; t < 4; ++t) acc[t] = f32x4{0.f, 0.f, 0.f, 0.f};
  const unsigned ONES = 0x3F803F80u;
  const v4u on4 = {ONES, ONES, ONES, ONES};
  u64 tag = 0;
  float rs[4], mn[4];
  if (tid == 0) *(LAS unsigned*)(L + L_CNT) = 0u;
  v4u f[2][8], fc[2];
  auto mm = [&](int i, int n) {
    const int ss = n / 6, r = n % 6;
    if (n == 12) acs = mfma(on4, fc[i & 1], acs);
    else if (r == 0) as1 = mfma(xf[2 * i + ss], on4, as1);
    else if (r == 1) as2 = mfma(xf[2 * i + ss], xf[2 * i + ss], as2);
    else acc[r - 2] = mfma(xf[2 * i + ss], f[i & 1][4 * ss + r - 2], acc[r - 2]);
  };
  auto rd = [&](int i, int n) {
    if (n == 0) xf[2 * i] = *(LAS v4u*)(L + (i & 3) * 16 * K + lx0);
    else if (n == 5) xf[2 * i + 1] = *(LAS v4u*)(L + (i & 3) * 16 * K + lx1);
    else if (n == 10) fc[i & 1] = trfrag(L, csa + i * 8192, 256);
    else {
      const int q = n < 5 ? n - 1 : n - 2;
      f[i & 1][q] = trfrag(L, ((q & 1) ? lq1 : lq0) + L_KV + (2 * i + (q >> 2)) * 4096 + ((q >> 1) & 1) * 2048, 256);
    }
  };
  float cs0[4];
  v4u g2a, g2b, fc6;
#pragma unroll
  for (int i = 0; i < 7; ++i) {
    constexpr int NW[7] = {2, 7, 6, 6, 7, 6, 4};
    asm volatile("s_waitcnt vmcnt(%2) lgkmcnt(0)\n\ts_barrier" : "+v"(ep), "+v"(cpf) : "i"(NW[i]) : "memory");
    if (i == 0) STAMP(13);
    if (i == 3) STAMP(16);
    if (i == 6) STAMP(9);
#pragma unroll
    for (int n = 0; n < 13; ++n) {
      if (n < (i < 6 ? 11 : 10)) rd(i, n);
      if (i > 0 && i < 6) mm(i - 1, n);
      if (i == 0) {
        if (n == 0) stage_issue(1);
        if (n == 1) issue_w(2);
        if (n == 2) issue_w(3);
        if (n == 3) issue_xa(2);
        if (n == 4) issue_xb(2);
        if (n == 5) issue_w(4);
        if (n == 6) issue_xa(3);
        if (n == 7) issue_xb(3);
      }
      if (i == 1 || i == 2) {
        if (n == 1) issue_w(i + 4);
        if (n == 5) issue_xa(i + 3);
        if (n == 9) issue_xb(i + 3);
      }
      if (i == 3) {
        if (n == 1) issue_xa(6);
        if (n == 4) issue_xb(6);
        if (n == 7) dma16(rA, v1off, rk1 * 32 * 3584, lbase + L_RP + 8 * K + wc * 1024);
        if (n == 10) dma16(rB, v8 - ct * 128, (32 * (rb2 % 56) + 8 * (w & 3)) * 896 + (rb2 / 56) * 128, lbase + L_RP + w * 1024);
      }
      if (i == 4 || i == 5) {
        if (n == 1) issue_q(2 * i - 8);
        if (n == 7) issue_q(2 * i - 7);
      }
      if (i == 5 && n == 11) fc6 = trfrag(L, csa + 6 * 8192, 256);
      if (i == 6 && n >= 4 && n < 12) mm(6, n < 8 ? (n & 1) + 6 * ((n - 4) >> 1) : n - 6);
      SB();
    }
    if (i == 5) {
      mm(5, 12);
      acs = mfma(on4, fc6, acs);
#pragma unroll
      for (int n = 0; n < 6; ++n) mm(5, n);
      if (g == 0) *(LAS float*)(L + L_CS1 + w * 64 + c * 4) = acs[0];
#pragma unroll
      for (int n = 6; n < 12; ++n) mm(5, n);
    }
    if (i == 2) {
      const u64 e = ((u64)(unsigned)__builtin_amdgcn_readfirstlane(ep.y) << 32) | (unsigned)__builtin_amdgcn_readfirstlane(ep.x);
      tag = e * 6364136223846793005ull + 1442695040888963407ull;
    }
    SB();
  }
  float dg1, dg2;
  {
#pragma unroll
    for (int t = 0; t < 4; ++t) cs0[t] = *(LAS float*)(L + L_CS1 + t * 64 + c * 4) + *(LAS float*)(L + L_CS1 + (t + 4) * 64 + c * 4);
    {
      const unsigned ra = ((l >> 5) * 8 + (g & 1) * 4 + (c >> 2)) * 224 + (c & 3) * 8 + L_RP + 8 * K;
      g2a = trfrag(L, ra + (w & 3) * 64, 3584);
      g2b = trfrag(L, ra + ((w & 3) == 3 ? 192 : (w & 3) * 64 + 32), 3584);
      r2f = trfrag(L, (l8 ^ ((w & 3) * 32)) + L_RP + (w >> 2) * 4096, 2048);
    }
    const bool b0 = c & 1, b1 = c & 2, dg = g == (c >> 2);
    dg2 = b1 ? (b0 ? as2[3] : as2[2]) : (b0 ? as2[1] : as2[0]);
    dg2 = dg ? dg2 : 0.f;
    const f32x4 a2 = rowsum(dg2);
    SB();
#pragma unroll
    for (int n = 8; n < 12; ++n) mm(6, n);
#pragma unroll
    for (int r = 0; r < 4; ++r) {
      mn[r] = as1[r] * (-1.0f / 448.0f);
      rs[r] = __builtin_amdgcn_rsqf(fmaxf(__builtin_fmaf(a2[r], 1.0f / 448.0f, -mn[r] * mn[r]), 0.f) + 1e-5f);
    }
    dg1 = b1 ? (b0 ? as1[3] : as1[2]) : (b0 ? as1[1] : as1[0]);
    dg1 = dg ? dg1 : 0.f;
  }
  STAMP(2);
#pragma unroll
  for (int t = 0; t < 4; ++t) {
    float v[4];
#pragma unroll
    for (int r = 0; r < 4; ++r) {
      const float d = __builtin_fmaf(mn[r], cs0[t], acc[t][r]);
      v[r] = t < 2 ? phi(d, rs[r], rs[r] * LOG2E) : d * rs[r];
      if (t == 3) v[r] = c < 4 ? 1.0f : v[r];
      OPQ(v[r]);
    }
    *(LAS v2u*)(L + xw + t * 4096) = v2u{pack2(v[0], v[1]), pack2(v[2], v[3])};
  }
  BAR_VM();
  STAMP(3);
  if (w < 4) {
    f32x4 kv = {0.f, 0.f, 0.f, 0.f};
    v4u a[4], bb[4];
#pragma unroll
    for (int ks = 0; ks < 4; ++ks) {
      a[ks] = *(LAS v4u*)(L + L_X + (((w >> 1) * 4 + ks) * 64 + l) * 16);
      bb[ks] = *(LAS v4u*)(L + L_X + (((2 + (w & 1)) * 4 + ks) * 64 + l) * 16);
    }
#pragma unroll
    for (int ks = 0; ks < 4; ++ks) kv = mfma(a[ks], bb[ks], kv);
    if (w >= 2 && g == 0) kv = f32x4{0.f, 0.f, 0.f, 0.f};
    st_sc1(__builtin_bit_cast(v4u, kv), rW, l * 16, WS_KVP + ((h * 4 + w) * 16 + i1) * 1024);
    st_sc1(g2a, rW, l * 16, WS_R1 + ((h * 14 + rk1) * 7 + 2 * w) * 1024);
    st_sc1(g2b, rW, l * 16, WS_R1 + ((h * 14 + rk1) * 7 + (w == 3 ? 6 : 2 * w + 1)) * 1024);
  }
  v4u pqf;
  v2u xr0, xr1, bb1;
  {
    constexpr int QB = 12;
    f32x4 aq[2] = {f32x4{0.f, 0.f, 0.f, 0.f}, f32x4{0.f, 0.f, 0.f, 0.f}};
    f32x4 csq = {0.f, 0.f, 0.f, 0.f};
    unsigned lqa = lq0 + L_Q, lqb = lq1 + L_Q;
    OPQ(lqa);
    OPQ(lqb);
    const unsigned qa = (lqa ^ ((w & 1) * 32)) + (w >> 1) * 2048;
    v4u f[28], fc[4];
#pragma unroll
    for (int q = 0; q < 6; ++q) f[q] = trfrag(L, ((q & 1) ? lqb : lqa) + (q >> 1) * 2048, 256);
    SB();
    const unsigned on3 = w < 4 ? ONES : 0u;
    const v4u on43 = {on3, on3, on3, on3};
    unsigned kvo[4];
#pragma unroll
    for (int n = 0; n < 4; ++n) kvo[n] = WS_KVP + ((h * 4 + (w & 3)) * 16 + 8 * (w >> 2) + 2 * ((n + sa) & 3)) * 1024;
    u64 early = 0;
#pragma unroll
    for (int q = 0; q < 28; ++q) {
      if (q + 6 < 28) f[q + 6] = trfrag(L, ((q & 1) ? lqb : lqa) + ((q + 6) >> 1) * 2048, 256);
      if (q < 4) fc[q] = trfrag(L, qa + (q < 3 ? q * 8192 : (w < 4 ? 3 * 8192 : 0)), 256);
      aq[q & 1] = mfma(f[q], xf[q >> 1], aq[q & 1]);
      if (q >= 6 && q < 10) csq = mfma(q < 9 ? on4 : on43, fc[q - 6], csq);
      if (q == QB && w < 4) {
        DRAIN();
        if (l == 0 && __hip_atomic_fetch_add((LAS unsigned*)(L + L_CNT), 1u, __ATOMIC_RELAXED, __HIP_MEMORY_SCOPE_WORKGROUP) == 3u)
          set_flag(ws, WS_F1 + (h * 16 + i1) * FS, tag);
        STAMP(4);
      }
      if (q % 4 == 0 && q < 20 && w >= 4 && w < 7) {
        const int gi = wrap((w - 4) * 5 + q / 4 + 4 * sa, 14);
        if (w < 6 || q < 16) dma16x4<8 * 896>(rO, v8, gi * 32 * 896, lbase + L_WO + gi * 4096);
      }
      if (q == 18 && g == 0) *(LAS float*)(L + L_CSQ + w * 64 + c * 4) = csq[0];
      if (q == 24 && w == 7) early = __hip_atomic_load((const GAS u64*)(ws + WS_F1 + (h * 16 + c) * FS), __ATOMIC_RELAXED, __HIP_MEMORY_SCOPE_AGENT);
      SB();
    }
    const float mq = colsum(dg1) * (-1.0f / 448.0f);
    const float rq = __builtin_amdgcn_rsqf(fmaxf(__builtin_fmaf(colsum(dg2), 1.0f / 448.0f, -mq * mq), 0.f) + 1e-5f);
    STAMP(5);
    if (w == 7 && !__all(early == tag)) poll_flags(ws, WS_F1 + h * 16 * FS, c, tag);
    BAR();
    STAMP(6);
    v4u p[8];
#pragma unroll
    for (int n = 0; n < 8; ++n) {
      p[n] = ld_sc1(rW, l * 16 + (n & 1) * 1024, kvo[n >> 1]);
      SB();
    }
    st_sc1(r2f, rW, l * 16, WS_R2 + (rb2 * 4 + (w & 3)) * 1024);
    const unsigned vxr = (64 * u + 16 * mt + c) * 896 + (64 * ct + 32 * kh + 4 * g) * 2;
    xr0 = __builtin_amdgcn_raw_buffer_load_b64(rX, vxr, 0, 0);
    xr1 = __builtin_amdgcn_raw_buffer_load_b64(rX, vxr + 32, 0, 0);
    bb1 = *(const GAS v2u*)(b1 + 112 * h + 16 * ((h & 1) ? (wc == 6 ? 0 : wc + 1) : wc) + 4 * g);
    SB();
#pragma unroll
    for (int t = 0; t < 2; ++t) {
      f32x4 cs = *(LAS f32x4*)(L + L_CSQ + t * 64 + g * 16);
#pragma unroll
      for (int m = 1; m < 4; ++m) cs += *(LAS f32x4*)(L + L_CSQ + (t + 2 * m) * 64 + g * 16);
      float v[4];
#pragma unroll
      for (int r = 0; r < 4; ++r) v[r] = phi(__builtin_fmaf(mq, cs[r], aq[t][r]), rq, rq * LOG2E);
      pqf[2 * t] = pack2(v[0], v[1]);
      pqf[2 * t + 1] = pack2(v[2], v[3]);
    }
    asm volatile("" : "+v"(pqf));
    SB();
    f32x4 sum = __builtin_bit_cast(f32x4, p[0]);
#pragma unroll
    for (int n = 1; n < 8; ++n) sum += __builtin_bit_cast(f32x4, p[n]);
    *(LAS f32x4*)(L + L_KVS + w * 1024 + l * 16) = sum;
  }
  BAR();
  STAMP(7);
  {
    f32x4 T[4], Z[2];
#pragma unroll
    for (int dt = 0; dt < 2; ++dt)
      Z[dt] = *(LAS f32x4*)(L + L_KVS + (dt * 2 + 1) * 1024 + g * 256) + *(LAS f32x4*)(L + L_KVS + (dt * 2 + 5) * 1024 + g * 256);
#pragma unroll
    for (int t = 0; t < 4; ++t) T[t] = *(LAS f32x4*)(L + L_KVS + t * 1024 + l * 16) + *(LAS f32x4*)(L + L_KVS + (t + 4) * 1024 + l * 16);
    v4u f0 = {pack2(T[0].x, T[0].y), pack2(T[0].z, T[0].w), pack2(T[2].x, T[2].y), pack2(T[2].z, T[2].w)};
    v4u f1 = {pack2(T[1].x, T[1].y), pack2(T[1].z, T[1].w), pack2(T[3].x, T[3].y), pack2(T[3].z, T[3].w)};
    v4u fz = {pack2(Z[0].x, Z[0].y), pack2(Z[0].z, Z[0].w), pack2(Z[1].x, Z[1].y), pack2(Z[1].z, Z[1].w)};
    const f32x4 z4 = {0.f, 0.f, 0.f, 0.f};
    f32x4 den = mfma(fz, pqf, z4), n0 = mfma(f0, pqf, z4), n1 = mfma(f1, pqf, z4);
    const float rd = __builtin_amdgcn_rcpf(den.x + 1e-6f);
    const float rd1 = g == 0 ? 0.f : rd;
    v4u of = {pack2(n0.x * rd, n0.y * rd), pack2(n0.z * rd, n0.w * rd), pack2(n1.x * rd1, n1.y * rd1), pack2(n1.z * rd1, n1.w * rd1)};
    st_sc1(of, rW, l * 16, WS_O + ((8 * i1 + w) * 16 + h) * 1024);
  }
  BAR_VM();
  if (tid == 0) {
    set_flag(ws, WS_F2 + (i1 * 16 + h) * FS, tag);
    set_flag(ws, WS_FR + b * 8, tag);
  }
  STAMP(8);
  asm volatile("" : "+v"(xr0), "+v"(xr1), "+v"(bb1));
  unsigned w1A = WS_R1 + ((h * 14 + r3) * 7 + w) * 1024, w1B = w1A - 14 * 7168, w1D = lbase + L_W1 + w * 1024;
  asm volatile("" : "+s"(w1A), "+s"(w1B), "+s"(w1D));
  auto w1dma = [&](int s) {
    dma16s(rW, l * 16, (r3 < 14 - s ? w1A : w1B) + s * 7168, w1D + s * 7168);
  };
  constexpr int W1A = 8;
  if (w < 7) {
#pragma unroll
    for (int s = 0; s < W1A; ++s) w1dma(s);
  }
  v4u hres;
  if (act24) {
    unsigned wb[4];
#pragma unroll
    for (int j = 0; j < 4; ++j) wb[j] = (l8 ^ (kh * 64) ^ (j * 32)) + L_WO + kh * (224 * 128);
    auto wfrag = [&](int q) {
      const int n = q >> 2, jl = q & 3;
      const unsigned po = (((n >> 1) + ct) & 3) * (56 * 128) + (n & 1) * (28 * 128);
      return trfrag2(L, wb[jl ^ ((n & 1) * 2)] + po, wb[jl ^ (((n & 1) ^ 1) * 2)] + po + 12 * 128);
    };
    unsigned oo[4];
#pragma unroll
    for (int n = 0; n < 4; ++n) {
      oo[n] = WS_O + ((4 * u + mt) * 16 + 8 * kh + 2 * ((n + ct) & 3)) * 1024;
      asm volatile("" : "+s"(oo[n]));
    }
    v4u wf[32];
#pragma unroll
    for (int q = 0; q < 32; ++q) wf[q] = wfrag(q);
    const int m2 = 4 * u + mt;
    f32x4 a2[4] = {f32x4{bflo(xr0.x), bfhi(xr0.x), bflo(xr0.y), bfhi(xr0.y)}, f32x4{bflo(xr1.x), bfhi(xr1.x), bflo(xr1.y), bfhi(xr1.y)},
                   f32x4{0.f, 0.f, 0.f, 0.f}, f32x4{0.f, 0.f, 0.f, 0.f}};
    asm volatile("" : "+v"(a2[0]), "+v"(a2[1]));
    if (w == 7) poll_flags(ws, WS_F2 + (u >> 1) * 16 * FS, c, tag);
    BAR();
    STAMP(10);
    v4u of[8];
    auto oload = [&](int n) { of[n] = ld_sc1(rW, l * 16 + (n & 1) * 1024, oo[n >> 1]); };
#pragma unroll
    for (int n = 0; n < 8; ++n) {
      oload(n);
      SB();
    }
#pragma unroll
    for (int q = 0; q < 32; ++q) {
      if (q == 12) asm volatile("" : "+v"(of[3]), "+v"(of[4]), "+v"(of[5]), "+v"(of[6]), "+v"(of[7]));
      if (q >= 12 && q % 3 == 0 && W1A + (q - 12) / 3 < 14 && w < 7) w1dma(W1A + (q - 12) / 3);
      a2[q & 3] = mfma(wf[q], of[q >> 2], a2[q & 3]);
      SB();
    }
    *(LAS f32x4*)(L + L_EX + w * 2048 + l * 16) = a2[2];
    *(LAS f32x4*)(L + L_EX + w * 2048 + 1024 + l * 16) = a2[3];
    BAR();
    STAMP(11);
    a2[0] += *(LAS f32x4*)(L + L_EX + (w ^ 4) * 2048 + l * 16);
    a2[1] += *(LAS f32x4*)(L + L_EX + (w ^ 4) * 2048 + 1024 + l * 16);
    hres = v4u{pack2(a2[0].x, a2[0].y), pack2(a2[0].z, a2[0].w), pack2(a2[1].x, a2[1].y), pack2(a2[1].z, a2[1].w)};
    st_sc1(hres, rW, l * 16, WS_H + (m2 * 16 + 2 * ct + kh) * 1024);
    const f32x4 z4 = {0.f, 0.f, 0.f, 0.f};
    const f32x4 gm = mfma(hres, hres, z4);
    const float t1 = mfma(on4, hres, z4)[0];
    const float t2 = (c & 2) ? ((c & 1) ? gm[3] : gm[2]) : ((c & 1) ? gm[1] : gm[0]);
    if (g == (c >> 2)) st_sc1_64(v2u{asu(t1), asu(t2)}, rW, c * 8, WS_ST + (m2 * 16 + 2 * ct + kh) * 128);
    if (w == 6) {
#pragma unroll 1
      for (unsigned it = 0; it < SPIN_CAP; ++it) {
        const v4u fa = ld_sc1(rW, l * 32, WS_FR), fb = ld_sc1(rW, l * 32 + 16, WS_FR);
        const unsigned tl = (unsigned)tag, th = (unsigned)(tag >> 32);
        if (__all(fa.x == tl && fa.y == th && fa.z == tl && fa.w == th && fb.x == tl && fb.y == th && fb.z == tl && fb.w == th)) break;
        asm volatile("s_sleep 1" ::: "memory");
      }
    }
    BAR_VM();
    if (tid == 0) {
      set_flag(ws, WS_F3 + (u * 8 + ct) * FS, tag);
      if (b == 0) set_flag(ws, WS_EPOCH, tag);
    }
  } else {
    if (w < 7) {
#pragma unroll
      for (int s = W1A; s < 14; ++s) w1dma(s);
    }
    BAR_VM();
  }
  STAMP(12);
  v4u wf2[7][4];
  unsigned w2o[7];
#pragma unroll
  for (int kk = 0; kk < 7; ++kk) {
    w2o[kk] = WS_R2 + (ct * 56 + 7 * w + wrap(kk + r4, 7)) * 4096;
    asm volatile("" : "+s"(w2o[kk]));
  }
  auto w2load = [&](int i) { wf2[i >> 2][i & 3] = ld_sc1(rW, l * 16 + (i & 3) * 1024, w2o[i >> 2]); };
  unsigned lP3 = L_W1 + l * 16, lP3b = lP3 + 7 * 7168;
  OPQ(lP3b);
  {
    v4u f[14];
#pragma unroll
    for (int s = 0; s < 14; ++s) f[s] = *(LAS v4u*)(L + (s < 7 ? lP3 + s * 7168 : lP3b + (s - 7) * 7168) + wc * 1024);
    SB();
    f32x4 cs = {0.f, 0.f, 0.f, 0.f};
#pragma unroll
    for (int s = 0; s < 14; ++s) {
      cs = mfma(f[s], on4, cs);
      if (s < W2_A) w2load(s);
      SB();
    }
    if (c == 0) {
      *(LAS f32x4*)(L + L_C1 + wc * 64 + g * 16) = cs * GC;
      *(LAS f32x4*)(L + L_C1 + 448 + wc * 64 + g * 16) = f32x4{bflo(bb1.x), bfhi(bb1.x), bflo(bb1.y), bfhi(bb1.y)} * GC;
    }
  }
  const int m3 = 8 * i1 + w;
  {
    auto fs = [](int q) { return q < 56 ? q >> 2 : (q - 56) % 14; };
    auto fj = [](int q) { return q < 56 ? q & 3 : 4 + (q - 56) / 14; };
    auto frag = [&](int q) {
      const int s = fs(q), j = fj(q);
      return *(LAS v4u*)(L + (s < 7 ? lP3 + s * 7168 : lP3b + (s - 7) * 7168) + j * 1024);
    };
    constexpr int PF3 = 6;
    v4u f[98];
#pragma unroll
    for (int q = 0; q < PF3; ++q) f[q] = frag(q);
    unsigned ho[14];
#pragma unroll
    for (int s = 0; s < 14; ++s) {
      ho[s] = WS_H + (m3 * 16 + wrap(s + r3, 14)) * 1024;
      asm volatile("" : "+s"(ho[s]));
    }
    if (w == 7) poll_flags(ws, WS_F3 + i1 * 16 * FS, (c & 7) == 7 ? 0 : c, tag);
    BAR();
    STAMP(14);
    v4u hf[14];
    auto hld = [&](int s) { hf[s] = ld_sc1(rW, l * 16, ho[s]); };
    constexpr int HA = 14;
#pragma unroll
    for (int s = 0; s < HA; ++s) {
      hld(s);
      SB();
    }
    v2u st[4];
    f32x4 a3[7];
#pragma unroll
    for (int j = 0; j < 7; ++j) a3[j] = f32x4{0.f, 0.f, 0.f, 0.f};
#pragma unroll
    for (int q = 0; q < 56; ++q) {
      f[q + PF3] = frag(q + PF3);
      a3[fj(q)] = mfma(f[q], hf[fs(q)], a3[fj(q)]);
      if (q >= 8 && q < 12) st[q - 8] = ld_sc1_64(rW, c * 8 + g * 128 + (q - 8) * 512, WS_ST + m3 * 2048);
      if (q >= 32 && q % 4 == 0) w2load(W2_A + (q - 32) / 4);
      SB();
    }
    STAMP(20);
    float t1 = asf(st[0].x) + asf(st[1].x) + asf(st[2].x) + (g < 2 ? asf(st[3].x) : 0.f);
    float t2 = asf(st[0].y) + asf(st[1].y) + asf(st[2].y) + (g < 2 ? asf(st[3].y) : 0.f);
    const float mean2 = colsum(t1) * (1.0f / 448.0f);
    const float rs2 = __builtin_amdgcn_rsqf(fmaxf(__builtin_fmaf(colsum(t2), 1.0f / 448.0f, -mean2 * mean2), 0.f) + 1e-5f);
    const float nm2 = -mean2 * rs2, rs2c = rs2 * GC;
    const unsigned ksp = (7 * h + 1) >> 1;
    const unsigned ob = WS_H1 + (m3 * 56 + ksp) * 1024;
    unsigned pk[14];
    auto epi1 = [&](int j, int r, const f32x4& cs, const f32x4& bb) {
      return gelu_s(__builtin_fmaf(a3[j][r], rs2c, __builtin_fmaf(nm2, cs[r], bb[r])));
    };
#pragma unroll
    for (int jj = 0; jj < 7; ++jj) {
      f32x4 cs = *(LAS f32x4*)(L + L_C1 + jj * 64 + g * 16);
      f32x4 bb = *(LAS f32x4*)(L + L_C1 + 448 + jj * 64 + g * 16);
      SB();
      float y[4];
#pragma unroll
      for (int s = 0; s < 7; ++s) {
        const int q = 56 + jj * 7 + s;
        if (jj < 6) {
          if (q + PF3 < 98) f[q + PF3] = frag(q + PF3);
          a3[fj(q)] = mfma(f[q], hf[fs(q)], a3[fj(q)]);
          if ((s == 1 || s == 4) && 6 + jj * 2 + s / 4 < W2_B) w2load(W2_A + 6 + jj * 2 + s / 4);
        }
        if (s == 2 || s == 4) {
          y[s - 2] = epi1(jj, s - 2, cs, bb);
          y[s - 1] = epi1(jj, s - 1, cs, bb);
        }
        if (s == 3 || s == 5) pk[2 * jj + (s - 3) / 2] = pack2(y[s - 3], y[s - 2]);
        if (s != 2 && s != 4) SB();
      }
      if (jj & 1) st_sc1(v4u{pk[2 * jj - 2], pk[2 * jj - 1], pk[2 * jj], pk[2 * jj + 1]}, rW, l * 16 + (jj >> 1) * 1024, ob);
    }
    st_sc1_64(v2u{pk[12], pk[13]}, rW, l * 16, (h & 1) ? ob - 1016 : ob + 3072);
    STAMP(21);
    BAR_VM();
    if (tid == 0) set_flag(ws, WS_F4 + (i1 * 16 + h) * FS, tag);
  }
  STAMP(15);
  if (act24) {
#pragma unroll
    for (int i = W2_A + W2_B; i < 28; ++i) w2load(i);
    v4u bb2 = *(const GAS v4u*)(b2 + 64 * ct + (tid & 7) * 8);
    *(LAS v2u*)(L + L_HT + (16 * mt + c) * 144 + (32 * kh + 4 * g) * 2) = v2u{hres.x, hres.y};
    *(LAS v2u*)(L + L_HT + (16 * mt + c) * 144 + (32 * kh + 16 + 4 * g) * 2) = v2u{hres.z, hres.w};
    unsigned ko[7], mo[4];
#pragma unroll
    for (int kk = 0; kk < 7; ++kk) {
      ko[kk] = l * 16 + wrap(kk + r4, 7) * 1024;
      asm volatile("" : "+v"(ko[kk]));
    }
#pragma unroll
    for (int m = 0; m < 4; ++m) {
      mo[m] = WS_H1 + ((4 * u + ((m + ct) & 3)) * 56 + 7 * w) * 1024;
      asm volatile("" : "+s"(mo[m]));
    }
    if (w == 7) poll_flags(ws, WS_F4 + (u >> 1) * 16 * FS, c, tag);
    BAR();
    STAMP(17);
    v4u hf[28];
    auto hload = [&](int i) {
      const int m = i / 7, kk = i % 7;
      hf[i] = ld_sc1(rW, ko[kk], mo[m]);
    };
    constexpr int HB = 10;
#pragma unroll
    for (int i = 0; i < HB; ++i) {
      hload(i);
      SB();
    }
    f32x4 a4[4][4];
    auto ptw = [&](int m, int j) { *(LAS f32x4*)(L + L_PT + w * 17408 + ((m + ct) & 3) * (16 * 272) + c * 272 + g * 16 + j * 64) = a4[m][j]; };
#pragma unroll
    for (int m = 0; m < 4; ++m) {
#pragma unroll
      for (int j = 0; j < 4; ++j) a4[m][j] = f32x4{0.f, 0.f, 0.f, 0.f};
#pragma unroll
      for (int kk = 0; kk < 7; ++kk) {
        if (7 * m + kk + HB < 28) hload(7 * m + kk + HB);
        if (m > 0 && kk >= 1 && kk <= 4) ptw(m - 1, kk - 1);
#pragma unroll
        for (int j = 0; j < 4; ++j) a4[m][j] = mfma(wf2[kk][j], hf[7 * m + kk], a4[m][j]);
        SB();
      }
    }
#pragma unroll
    for (int j = 0; j < 4; ++j) ptw(3, j);
    BAR();
    STAMP(18);
    const unsigned pa = L_PT + (tid >> 3) * 272 + (tid & 7) * 32;
    f32x4 o0 = *(LAS f32x4*)(L + pa), o1 = *(LAS f32x4*)(L + pa + 16);
#pragma unroll
    for (int ww = 1; ww < 8; ++ww) {
      o0 += *(LAS f32x4*)(L + pa + ww * 17408);
      o1 += *(LAS f32x4*)(L + pa + ww * 17408 + 16);
    }
    v4u hh = *(LAS v4u*)(L + L_HT + (tid >> 3) * 144 + (tid & 7) * 16);
    constexpr float IG = 1.0f / GC;
    v4u ov = {pack2(o0.x * IG + (bflo(bb2.x) + bflo(hh.x)), o0.y * IG + (bfhi(bb2.x) + bfhi(hh.x))),
              pack2(o0.z * IG + (bflo(bb2.y) + bflo(hh.y)), o0.w * IG + (bfhi(bb2.y) + bfhi(hh.y))),
              pack2(o1.x * IG + (bflo(bb2.z) + bflo(hh.z)), o1.y * IG + (bfhi(bb2.z) + bfhi(hh.z))),
              pack2(o1.z * IG + (bflo(bb2.w) + bflo(hh.w)), o1.w * IG + (bfhi(bb2.w) + bfhi(hh.w)))};
    *(GAS v4u*)(out + (64 * u + (tid >> 3)) * 448 + 64 * ct + (tid & 7) * 8) = ov;
  }
  STAMP(19);
  asm volatile(".Lfb_end:" ::: "memory");
}

extern "C" void kernel_launch(void* const* d_in, const int* in_sizes, int n_in, void* d_out, int out_size, void* d_ws,
                              size_t ws_size, hipStream_t stream) {
  (void)in_sizes; (void)n_in; (void)out_size; (void)ws_size;
  fused_block<<<256, 512, 0, stream>>>((const u16*)d_in[0], (const u16*)d_in[1], (const u16*)d_in[2], (const u16*)d_in[4],
                                       (const u16*)d_in[5], (const u16*)d_in[6], (const u16*)d_in[7], (u16*)d_out,
                                       (unsigned char*)d_ws);
}
```

```cpp
#include <hip/hip_runtime.h>
#include <hip/hip_bf16.h>

#define LAS __attribute__((address_space(3)))
#define GAS __attribute__((address_space(1)))
#define DI __device__ __forceinline__
typedef unsigned v4u __attribute__((ext_vector_type(4)));
typedef unsigned v2u __attribute__((ext_vector_type(2)));
typedef float f32x4 __attribute__((ext_vector_type(4)));
typedef float f32x2 __attribute__((ext_vector_type(2)));
typedef short s16x4 __attribute__((ext_vector_type(4)));
typedef __bf16 bf16x8 __attribute__((ext_vector_type(8)));
typedef __bf16 bf16x2 __attribute__((ext_vector_type(2)));
typedef unsigned long long u64;
typedef unsigned short u16;
typedef __amdgpu_buffer_rsrc_t rsrc_t;

#define W2_A 6
#define HAV 14
#define QBV 10
#define HBV 10
#define W2_B 16

namespace {
constexpr int S_ = 2048, D_ = 448, F_ = 1792;
constexpr unsigned FS = 64;
constexpr unsigned WS_EPOCH = 0;
constexpr unsigned WS_F1 = 4096;
constexpr unsigned WS_F2 = WS_F1 + 256 * FS;
constexpr unsigned WS_F3 = WS_F2 + 256 * FS;
constexpr unsigned WS_F4 = WS_F3 + 256 * FS;
constexpr unsigned WS_FR = WS_F4 + 256 * FS;
constexpr unsigned WS_KVP = 131072;
constexpr unsigned WS_O = WS_KVP + (1u << 20);
constexpr unsigned WS_H = WS_O + (2u << 20);
constexpr unsigned WS_ST = WS_H + (2u << 20);
constexpr unsigned WS_H1 = WS_ST + (256u << 10);
constexpr unsigned WS_R1 = WS_H1 + 128u * 56u * 1024u;
constexpr unsigned WS_R2 = WS_R1 + 16u * 14u * 7168u;
constexpr unsigned WS_END = WS_R2 + 7u * 56u * 4096u;
constexpr unsigned WS_DBG = 32u << 20;
constexpr unsigned K = 1024;
constexpr unsigned L_KV = 0, L_Q = 56 * K, L_X = 84 * K, L_WO = 100 * K, L_SC = 156 * K;
constexpr unsigned L_CSQ = L_SC, L_CS1 = L_SC + 512, L_C1 = L_SC + 1024, L_CNT = L_SC + 2048;
constexpr unsigned L_EX = 104 * K, L_KVS = 88 * K, L_RP = 0;
constexpr unsigned L_W1 = 0, L_PT = 0, L_HT = 140 * K;
constexpr unsigned LDS_BYTES = 163840;
constexpr int AUX_SC1 = 16;
constexpr unsigned SPIN_CAP = 1u << 18;
constexpr unsigned xslot(int i) { return 88 * K + (i & 3) * 16 * K; }

DI f32x4 mfma(v4u a, v4u b, f32x4 c) {
  return __builtin_amdgcn_mfma_f32_16x16x32_bf16(__builtin_bit_cast(bf16x8, a), __builtin_bit_cast(bf16x8, b), c, 0, 0, 0);
}
DI f32x4 rowsum(float v) {
  asm volatile("s_nop 2" : "+v"(v));
  return __builtin_amdgcn_mfma_f32_16x16x4f32(v, 1.0f, f32x4{0.f, 0.f, 0.f, 0.f}, 0, 0, 0);
}
DI float colsum(float v) {
  asm volatile("s_nop 2" : "+v"(v));
  return __builtin_amdgcn_mfma_f32_16x16x4f32(1.0f, v, f32x4{0.f, 0.f, 0.f, 0.f}, 0, 0, 0)[0];
}
DI unsigned pack2(float a, float b) {
  bf16x2 v = {(__bf16)a, (__bf16)b};
  return __builtin_bit_cast(unsigned, v);
}
DI float asf(unsigned u) { return __builtin_bit_cast(float, u); }
DI unsigned asu(float f) { return __builtin_bit_cast(unsigned, f); }
DI float bflo(unsigned u) { return asf(u << 16); }
DI float bfhi(unsigned u) { return asf(u & 0xffff0000u); }
DI float dot2(unsigned a, unsigned b, float c) {
  return __builtin_amdgcn_fdot2_f32_bf16(__builtin_bit_cast(bf16x2, a), __builtin_bit_cast(bf16x2, b), c, false);
}
DI v4u trfrag2(LAS unsigned char* L, unsigned a, unsigned b) {
  s16x4 lo = __builtin_amdgcn_ds_read_tr16_b64_v4i16((LAS s16x4*)(L + a));
  s16x4 hi = __builtin_amdgcn_ds_read_tr16_b64_v4i16((LAS s16x4*)(L + b));
  v2u l2 = __builtin_bit_cast(v2u, lo), h2 = __builtin_bit_cast(v2u, hi);
  return v4u{l2.x, l2.y, h2.x, h2.y};
}
DI v4u trfrag(LAS unsigned char* L, unsigned a, unsigned dt) { return trfrag2(L, a, a + dt); }
DI void dma16(rsrc_t rs, unsigned voff, unsigned soff, unsigned ldst) {
  asm volatile("s_mov_b32 m0, %0\n\ts_nop 0\n\tbuffer_load_dwordx4 %1, %2, %3 offen lds" ::"s"(ldst), "v"(voff), "s"(rs), "s"(soff) : "memory");
}
DI void dma16s(rsrc_t rs, unsigned voff, unsigned soff, unsigned ldst) {
  asm volatile("s_mov_b32 m0, %0\n\ts_nop 0\n\tbuffer_load_dwordx4 %1, %2, %3 offen sc1 lds" ::"s"(ldst), "v"(voff), "s"(rs), "s"(soff) : "memory");
}
DI void dma16x2(rsrc_t rs, unsigned voff, unsigned voff2, unsigned soff, unsigned ldst) {
  asm volatile("s_mov_b32 m0, %0\n\ts_nop 0\n\tbuffer_load_dwordx4 %1, %3, %4 offen lds\n\tbuffer_load_dwordx4 %2, %3, %4 offen offset:1024 lds"
               ::"s"(ldst), "v"(voff), "v"(voff2), "s"(rs), "s"(soff) : "memory");
}
template <int STR> DI void dma16x4(rsrc_t rs, unsigned voff, unsigned soff, unsigned ldst) {
  unsigned t;
  asm volatile("s_mov_b32 m0, %1\n\ts_add_u32 %0, %4, %5\n\tbuffer_load_dwordx4 %2, %3, %4 offen lds\n\t"
               "buffer_load_dwordx4 %2, %3, %0 offen offset:1024 lds\n\ts_add_u32 %0, %0, %5\n\t"
               "buffer_load_dwordx4 %2, %3, %0 offen offset:2048 lds\n\ts_add_u32 %0, %0, %5\n\t"
               "buffer_load_dwordx4 %2, %3, %0 offen offset:3072 lds"
               : "=&s"(t) : "s"(ldst), "v"(voff), "s"(rs), "s"(soff), "i"(STR - 1024) : "memory", "scc");
}
#define BAR() asm volatile("s_waitcnt lgkmcnt(0)\n\ts_barrier" ::: "memory")
#define BAR_VM() asm volatile("s_waitcnt vmcnt(0) lgkmcnt(0)\n\ts_barrier" ::: "memory")
#define DRAIN() asm volatile("s_waitcnt vmcnt(0)" ::: "memory")
#define SB() __builtin_amdgcn_sched_barrier(0)
#define OPQ(x) asm("" : "+v"(x))
constexpr float LOG2E = 1.4426950408889634f;
DI float phi(float t, float rs, float rl) { return __builtin_amdgcn_exp2f(fminf(t * rl, 0.f)) + fmaxf(__builtin_fmaf(t, rs, 1e-6f), 1e-6f); }
constexpr float GC = 0.4686686930532063f, GK = -4.91222953926424f;
DI float gelu_s(float v) {
  float e = __builtin_amdgcn_exp2f(v * __builtin_fmaf(-v, v, GK));
  float y = v * __builtin_amdgcn_rcpf(1.0f + e);
  OPQ(y);
  return y;
}
DI void poll_flags(unsigned char* ws, unsigned off, int idx, u64 tag) {
  const GAS u64* q = (const GAS u64*)(ws + off + idx * FS);
#pragma unroll 1
  for (unsigned it = 0; it < SPIN_CAP; ++it) {
    u64 v = __hip_atomic_load(q, __ATOMIC_RELAXED, __HIP_MEMORY_SCOPE_AGENT);
    if (__all(v == tag)) break;
    __builtin_amdgcn_s_sleep(1);
  }
  __builtin_amdgcn_fence(__ATOMIC_ACQUIRE, "wavefront");
}
DI void set_flag(unsigned char* ws, unsigned off, u64 tag) {
  __hip_atomic_store((GAS u64*)(ws + off), tag, __ATOMIC_RELAXED, __HIP_MEMORY_SCOPE_AGENT);
}
DI v4u ld_sc1(rsrc_t rs, unsigned voff, unsigned soff) { return __builtin_amdgcn_raw_buffer_load_b128(rs, voff, soff, AUX_SC1); }
DI v2u ld_sc1_64(rsrc_t rs, unsigned voff, unsigned soff) { return __builtin_amdgcn_raw_buffer_load_b64(rs, voff, soff, AUX_SC1); }
DI void st_sc1(v4u v, rsrc_t rs, unsigned voff, unsigned soff) { __builtin_amdgcn_raw_buffer_store_b128(v, rs, voff, soff, AUX_SC1); }
DI void st_sc1_64(v2u v, rsrc_t rs, unsigned voff, unsigned soff) { __builtin_amdgcn_raw_buffer_store_b64(v, rs, voff, soff, AUX_SC1); }
DI int wrap(int v, int m) { return v >= m ? v - m : v; }

#define STAMP(k) do { } while (0)
}

__global__ void __launch_bounds__(512, 2)
fused_block(const u16* __restrict__ x, const u16* __restrict__ wqkv, const u16* __restrict__ wo, const u16* __restrict__ w1,
            const u16* __restrict__ b1, const u16* __restrict__ w2, const u16* __restrict__ b2, u16* __restrict__ out,
            unsigned char* ws) {
  __shared__ __attribute__((aligned(1024))) unsigned char smem[LDS_BYTES];
  LAS unsigned char* L = (LAS unsigned char*)smem;
  const unsigned lbase = (unsigned)(size_t)L;
  STAMP(0);
  const int tid = threadIdx.x, l = tid & 63, w = __builtin_amdgcn_readfirstlane(tid >> 6), g = l >> 4, c = l & 15;
  const int b = blockIdx.x, xcd = b & 7, slot = b >> 3;
  const int i1 = 4 * (xcd >> 1) + (slot >> 3), h = 8 * (xcd & 1) + (slot & 7);
  const int u = 4 * xcd + (slot >> 3), ct = slot & 7;
  const bool act24 = ct < 7;
  int m3 = 8 * i1 + w;
  asm volatile("" : "+s"(m3));
  const int sa = slot >> 3;
#define ROT 64
#define R1M 0
  const int r1 = R1M == 0 ? slot % 7 : R1M == 1 ? 0 : R1M == 2 ? 2 * sa : R1M == 3 ? (ct == 7 ? 0 : ct) : R1M == 4 ? (sa & 1) * 3 : R1M == 5 ? (ct & 1) * 3 : (ct >> 1) * 2, r3 = (ROT & 2) ? wrap(4 * sa + 2 * ct, 14) : 0, r4 = (ROT & 4) ? (2 * sa + ct) % 7 : 0;
  const int rka = (ROT & 8) ? sa : 0, roc = (ROT & 16) ? ct : 0, rmc = (ROT & 32) ? ct : 0, rwo = (ROT & 64) ? sa : 0;

  const rsrc_t rX = __builtin_amdgcn_make_buffer_rsrc((void*)x, 0, S_ * D_ * 2, 0x00020000);
  const rsrc_t rQ = __builtin_amdgcn_make_buffer_rsrc((void*)wqkv, 0, D_ * 3 * D_ * 2, 0x00020000);
  const rsrc_t rO = __builtin_amdgcn_make_buffer_rsrc((void*)wo, 0, D_ * D_ * 2, 0x00020000);
  const rsrc_t rA = __builtin_amdgcn_make_buffer_rsrc((void*)w1, 0, D_ * F_ * 2, 0x00020000);
  const rsrc_t rB = __builtin_amdgcn_make_buffer_rsrc((void*)w2, 0, F_ * D_ * 2, 0x00020000);
  const rsrc_t rW = __builtin_amdgcn_make_buffer_rsrc((void*)ws, 0, WS_END, 0x00020000);

  const unsigned vW = (l >> 2) * 2688 + (((l >> 1) & 1) ^ (l >> 5)) * 24 + (l & 1) * 16;
  const unsigned vXa = (16 * m3 + (l >> 3)) * 896 + ((l & 7) ^ g) * 16;
  const unsigned vXb = (vXa ^ 64) + 8 * 896;
  const unsigned sW = (32 * (w >> 2) + 16 * (w & 1)) * 2688 + (1 + ((w >> 1) & 1)) * 896 + 56 * h;
  auto issue_w = [&](int i) { dma16(rQ, vW, sW + wrap(i + r1, 7) * 64 * 2688, lbase + L_KV + w * 1024 + i * 8192); };
  auto issue_xa = [&](int i) { dma16(rX, vXa, wrap(i + r1, 7) * 128, lbase + xslot(i) + w * 2048); };
  auto issue_xb = [&](int i) { dma16(rX, vXb, wrap(i + r1, 7) * 128, lbase + xslot(i) + w * 2048 + 1024); };
  auto issue_q = [&](int m) {
    int n = w + 8 * m;
    if (m == 3) n = n >= 28 ? n - 4 : n;
    dma16(rQ, vW, (64 * wrap((n >> 2) + r1, 7) + 16 * (n & 3)) * 2688 + 56 * h, lbase + L_Q + n * 1024);
  };
  auto stage_issue = [&](int i) {
    issue_w(i);
    issue_xa(i);
    issue_xb(i);
  };
  stage_issue(0);
  const unsigned v8 = (l >> 3) * 896 + (((l >> 1) & 3) ^ g) * 32 + (l & 1) * 16 + ct * 128;
  unsigned v1off;
  {
    const int ch = 64 * (w < 7 ? w : 6) + l, row = (ch * 2341) >> 15, cc = ch - 14 * row, js = cc >> 1;
    const int j = (h & 1) ? (js == 6 ? 0 : js + 1) : js;
    v1off = row * 3584 + (112 * h + 16 * j) * 2 + (cc & 1) * 16;
  }
  const int wc = w < 7 ? w : 6;
  const int mt = w & 3, kh = w >> 2;
  unsigned l8 = (l >> 5) * 1024 + ((g & 1) * 4 + (c >> 2)) * 128 + ((g & 1) * 2 + (c >> 3)) * 32 + (c & 3) * 8;
  const int rk1 = i1 < 14 ? i1 : 13, rb2 = w < 4 || b >= 136 ? b : b + 256;
  v4u r2f;
  unsigned cpf;
  v2u ep;
  {
    u64 pc;
    unsigned sz;
    asm volatile(".Lfb_start:\n\ts_getpc_b64 %0\n\ts_mov_b32 %1, .Lfb_end-.Lfb_start" : "=s"(pc), "=s"(sz));
    const rsrc_t rC = __builtin_amdgcn_make_buffer_rsrc((void*)pc, 0, sz, 0x00020000);
    asm volatile("s_nop 4\n\tbuffer_load_dword %0, %1, %2, 0 offen" : "=v"(cpf) : "v"(w == 7 ? ((slot & 3) * 64 + l) * 128 : 0x7FFFFFF0), "s"(rC) : "memory");
    asm volatile("buffer_load_dwordx2 %0, off, %1, 0 sc1" : "=v"(ep) : "s"(rW) : "memory");
  }
  STAMP(1);
  const unsigned lq0 = (l >> 5) * 1024 + (g & 1) * 544 + (c >> 2) * 64 + (c & 3) * 8, lq1 = lq0 ^ 32;
  unsigned lx0 = xslot(0) + w * 2048 + c * 128 + (g ^ (c >> 1)) * 16, lx1 = lx0 ^ 64;
  OPQ(lx0);
  OPQ(lx1);
  const unsigned xw = L_X + (w >> 1) * 1024 + l * 16 + (w & 1) * 8;
  v4u xf[14];
  f32x4 acc[4], as1 = {0.f, 0.f, 0.f, 0.f}, as2 = as1, acs = as1;
  const unsigned csa = (lq0 ^ ((w & 1) * 32)) + L_KV + (w >> 2) * 4096 + ((w >> 1) & 1) * 2048;
#pragma unroll
  for (int t = 0; t < 4; ++t) acc[t] = f32x4{0.f, 0.f, 0.f, 0.f};
  const unsigned ONES = 0x3F803F80u;
  const v4u on4 = {ONES, ONES, ONES, ONES};
  u64 tag = 0;
  float rs[4], mn[4];
  if (tid == 0) *(LAS unsigned*)(L + L_CNT) = 0u;
  v4u f[2][8], fc[2];
  auto mm = [&](int i, int n) {
    const int ss = n / 6, r = n % 6;
    if (n == 12) acs = mfma(on4, fc[i & 1], acs);
    else if (r == 0) as1 = mfma(xf[2 * i + ss], on4, as1);
    else if (r == 1) as2 = mfma(xf[2 * i + ss], xf[2 * i + ss], as2);
    else acc[r - 2] = mfma(xf[2 * i + ss], f[i & 1][4 * ss + r - 2], acc[r - 2]);
  };
  auto rd = [&](int i, int n) {
    if (n == 0) xf[2 * i] = *(LAS v4u*)(L + (i & 3) * 16 * K + lx0);
    else if (n == 5) xf[2 * i + 1] = *(LAS v4u*)(L + (i & 3) * 16 * K + lx1);
    else if (n == 10) fc[i & 1] = trfrag(L, csa + i * 8192, 256);
    else {
      const int q = n < 5 ? n - 1 : n - 2;
      f[i & 1][q] = trfrag(L, ((q & 1) ? lq1 : lq0) + L_KV + (2 * i + (q >> 2)) * 4096 + ((q >> 1) & 1) * 2048, 256);
    }
  };
  float cs0[4];
  v4u g2a, fc6;
#pragma unroll
  for (int i = 0; i < 7; ++i) {
    constexpr int NW[7] = {2, 7, 6, 6, 7, 6, 4};
    asm volatile("s_waitcnt vmcnt(%2) lgkmcnt(0)\n\ts_barrier" : "+v"(ep), "+v"(cpf) : "i"(NW[i]) : "memory");
    if (i == 0) STAMP(13);
    if (i == 3) STAMP(16);
    if (i == 6) STAMP(9);
#pragma unroll
    for (int n = 0; n < 13; ++n) {
      if (n < (i < 6 ? 11 : 10)) rd(i, n);
      if (i > 0 && i < 6) mm(i - 1, n);
      if (i == 0) {
        if (n == 0) stage_issue(1);
        if (n == 1) issue_w(2);
        if (n == 2) issue_w(3);
        if (n == 3) issue_xa(2);
        if (n == 4) issue_xb(2);
        if (n == 5) issue_w(4);
        if (n == 6) issue_xa(3);
        if (n == 7) issue_xb(3);
      }
      if (i == 1 || i == 2) {
        if (n == 1) issue_w(i + 4);
        if (n == 5) issue_xa(i + 3);
        if (n == 9) issue_xb(i + 3);
      }
      if (i == 3) {
        if (n == 1) issue_xa(6);
        if (n == 4) issue_xb(6);
        if (n == 7) dma16(rA, v1off, rk1 * 32 * 3584, lbase + L_RP + 8 * K + wc * 1024);
        if (n == 10) dma16(rB, v8 - ct * 128, (32 * (rb2 % 56) + 8 * (w & 3)) * 896 + (rb2 / 56) * 128, lbase + L_RP + w * 1024);
      }
      if (i == 4 || i == 5) {
        if (n == 1) issue_q(2 * i - 8);
        if (n == 7) issue_q(2 * i - 7);
      }
      if (i == 5 && n == 11) fc6 = trfrag(L, csa + 6 * 8192, 256);
      if (i == 6 && n >= 4 && n < 12) mm(6, n < 8 ? (n & 1) + 6 * ((n - 4) >> 1) : n - 6);
      SB();
    }
    if (i == 5) {
      mm(5, 12);
      acs = mfma(on4, fc6, acs);
#pragma unroll
      for (int n = 0; n < 6; ++n) mm(5, n);
      if (g == 0) *(LAS float*)(L + L_CS1 + w * 64 + c * 4) = acs[0];
#pragma unroll
      for (int n = 6; n < 12; ++n) mm(5, n);
    }
    if (i == 2) {
      const u64 e = ((u64)(unsigned)__builtin_amdgcn_readfirstlane(ep.y) << 32) | (unsigned)__builtin_amdgcn_readfirstlane(ep.x);
      tag = e * 6364136223846793005ull + 1442695040888963407ull;
    }
    SB();
  }
  float dg1, dg2;
  {
#pragma unroll
    for (int t = 0; t < 4; ++t) cs0[t] = *(LAS float*)(L + L_CS1 + t * 64 + c * 4) + *(LAS float*)(L + L_CS1 + (t + 4) * 64 + c * 4);
    {
      const unsigned ra = ((l >> 5) * 8 + (g & 1) * 4 + (c >> 2)) * 224 + (c & 3) * 8 + L_RP + 8 * K;
      g2a = trfrag(L, ra + wc * 32, 3584);
      r2f = trfrag(L, (l8 ^ ((w & 3) * 32)) + L_RP + (w >> 2) * 4096, 2048);
    }
    const bool b0 = c & 1, b1 = c & 2, dg = g == (c >> 2);
    dg2 = b1 ? (b0 ? as2[3] : as2[2]) : (b0 ? as2[1] : as2[0]);
    dg2 = dg ? dg2 : 0.f;
    const f32x4 a2 = rowsum(dg2);
    SB();
#pragma unroll
    for (int n = 8; n < 12; ++n) mm(6, n);
#pragma unroll
    for (int r = 0; r < 4; ++r) {
      mn[r] = as1[r] * (-1.0f / 448.0f);
      rs[r] = __builtin_amdgcn_rsqf(fmaxf(__builtin_fmaf(a2[r], 1.0f / 448.0f, -mn[r] * mn[r]), 0.f) + 1e-5f);
    }
    dg1 = b1 ? (b0 ? as1[3] : as1[2]) : (b0 ? as1[1] : as1[0]);
    dg1 = dg ? dg1 : 0.f;
  }
  STAMP(2);
#pragma unroll
  for (int t = 0; t < 4; ++t) {
    float v[4];
#pragma unroll
    for (int r = 0; r < 4; ++r) {
      const float d = __builtin_fmaf(mn[r], cs0[t], acc[t][r]);
      v[r] = t < 2 ? phi(d, rs[r], rs[r] * LOG2E) : d * rs[r];
      if (t == 3) v[r] = c < 4 ? 1.0f : v[r];
      OPQ(v[r]);
    }
    *(LAS v2u*)(L + xw + t * 4096) = v2u{pack2(v[0], v[1]), pack2(v[2], v[3])};
  }
  BAR_VM();
  STAMP(3);
  if (w < 4) {
    f32x4 kv = {0.f, 0.f, 0.f, 0.f};
    v4u a[4], bb[4];
#pragma unroll
    for (int ks = 0; ks < 4; ++ks) {
      a[ks] = *(LAS v4u*)(L + L_X + (((w >> 1) * 4 + ks) * 64 + l) * 16);
      bb[ks] = *(LAS v4u*)(L + L_X + (((2 + (w & 1)) * 4 + ks) * 64 + l) * 16);
    }
#pragma unroll
    for (int ks = 0; ks < 4; ++ks) kv = mfma(a[ks], bb[ks], kv);
    if (w >= 2 && g == 0) kv = f32x4{0.f, 0.f, 0.f, 0.f};
    st_sc1(__builtin_bit_cast(v4u, kv), rW, l * 16, WS_KVP + ((h * 4 + w) * 16 + i1) * 1024);
  }
  v4u pqf;
  v2u xr0, xr1, bb1;
  {
    constexpr int QB = QBV;
    f32x4 aq[2] = {f32x4{0.f, 0.f, 0.f, 0.f}, f32x4{0.f, 0.f, 0.f, 0.f}};
    f32x4 csq = {0.f, 0.f, 0.f, 0.f};
    unsigned lqa = lq0 + L_Q, lqb = lq1 + L_Q;
    OPQ(lqa);
    OPQ(lqb);
    const unsigned qa = (lqa ^ ((w & 1) * 32)) + (w >> 1) * 2048;
    v4u f[28], fc[4];
#pragma unroll
    for (int q = 0; q < 6; ++q) f[q] = trfrag(L, ((q & 1) ? lqb : lqa) + (q >> 1) * 2048, 256);
    SB();
    const unsigned on3 = w < 4 ? ONES : 0u;
    const v4u on43 = {on3, on3, on3, on3};
    unsigned kvo[4];
#pragma unroll
    for (int n = 0; n < 4; ++n) kvo[n] = WS_KVP + ((h * 4 + (w & 3)) * 16 + 8 * (w >> 2) + 2 * ((n + rka) & 3)) * 1024;
    u64 early = 0;
#pragma unroll
    for (int q = 0; q < 28; ++q) {
      if (q + 6 < 28) f[q + 6] = trfrag(L, ((q & 1) ? lqb : lqa) + ((q + 6) >> 1) * 2048, 256);
      if (q < 4) fc[q] = trfrag(L, qa + (q < 3 ? q * 8192 : (w < 4 ? 3 * 8192 : 0)), 256);
      aq[q & 1] = mfma(f[q], xf[q >> 1], aq[q & 1]);
      if (q >= 6 && q < 10) csq = mfma(q < 9 ? on4 : on43, fc[q - 6], csq);
      if (q == QB && w < 4) {
        DRAIN();
        if (l == 0 && __hip_atomic_fetch_add((LAS unsigned*)(L + L_CNT), 1u, __ATOMIC_RELAXED, __HIP_MEMORY_SCOPE_WORKGROUP) == 3u)
          set_flag(ws, WS_F1 + (h * 16 + i1) * FS, tag);
        STAMP(4);
      }
      if (q % 4 == 0 && q < 20 && w >= 4 && w < 7) {
        const int gi = wrap((w - 4) * 5 + q / 4 + 4 * rwo, 14);
        if (w < 6 || q < 16) dma16x4<8 * 896>(rO, v8, gi * 32 * 896, lbase + L_WO + gi * 4096);
      }
      if (q == 18 && g == 0) *(LAS float*)(L + L_CSQ + w * 64 + c * 4) = csq[0];
      if (q == 24 && w == 7) early = __hip_atomic_load((const GAS u64*)(ws + WS_F1 + (h * 16 + c) * FS), __ATOMIC_RELAXED, __HIP_MEMORY_SCOPE_AGENT);
      SB();
    }
    auto rstore = [&]() {
      st_sc1(g2a, rW, l * 16, WS_R1 + ((h * 14 + rk1) * 7 + wc) * 1024);
      st_sc1(r2f, rW, l * 16, WS_R2 + (rb2 * 4 + (w & 3)) * 1024);
    };
    if (w < 7) rstore();
    const float mq = colsum(dg1) * (-1.0f / 448.0f);
    const float rq = __builtin_amdgcn_rsqf(fmaxf(__builtin_fmaf(colsum(dg2), 1.0f / 448.0f, -mq * mq), 0.f) + 1e-5f);
    STAMP(5);
    if (w == 7 && !__all(early == tag)) poll_flags(ws, WS_F1 + h * 16 * FS, c, tag);
    BAR();
    STAMP(6);
    v4u p[8];
#pragma unroll
    for (int n = 0; n < 8; ++n) {
      p[n] = ld_sc1(rW, l * 16 + (n & 1) * 1024, kvo[n >> 1]);
      SB();
    }
    if (w == 7) rstore();
    const unsigned vxr = (64 * u + 16 * mt + c) * 896 + (64 * ct + 32 * kh + 4 * g) * 2;
    xr0 = __builtin_amdgcn_raw_buffer_load_b64(rX, vxr, 0, 0);
    xr1 = __builtin_amdgcn_raw_buffer_load_b64(rX, vxr + 32, 0, 0);
    bb1 = *(const GAS v2u*)(b1 + 112 * h + 16 * ((h & 1) ? (wc == 6 ? 0 : wc + 1) : wc) + 4 * g);
    SB();
#pragma unroll
    for (int t = 0; t < 2; ++t) {
      f32x4 cs = *(LAS f32x4*)(L + L_CSQ + t * 64 + g * 16);
#pragma unroll
      for (int m = 1; m < 4; ++m) cs += *(LAS f32x4*)(L + L_CSQ + (t + 2 * m) * 64 + g * 16);
      float v[4];
#pragma unroll
      for (int r = 0; r < 4; ++r) v[r] = phi(__builtin_fmaf(mq, cs[r], aq[t][r]), rq, rq * LOG2E);
      pqf[2 * t] = pack2(v[0], v[1]);
      pqf[2 * t + 1] = pack2(v[2], v[3]);
    }
    asm volatile("" : "+v"(pqf));
    SB();
    f32x4 sum = __builtin_bit_cast(f32x4, p[0]);
#pragma unroll
    for (int n = 1; n < 8; ++n) sum += __builtin_bit_cast(f32x4, p[n]);
    *(LAS f32x4*)(L + L_KVS + w * 1024 + l * 16) = sum;
  }
  BAR_VM();
  if (tid == 0) set_flag(ws, WS_FR + (h * 16 + i1) * 8, tag);
  STAMP(7);
  {
    f32x4 T[4], Z[2];
#pragma unroll
    for (int dt = 0; dt < 2; ++dt)
      Z[dt] = *(LAS f32x4*)(L + L_KVS + (dt * 2 + 1) * 1024 + g * 256) + *(LAS f32x4*)(L + L_KVS + (dt * 2 + 5) * 1024 + g * 256);
#pragma unroll
    for (int t = 0; t < 4; ++t) T[t] = *(LAS f32x4*)(L + L_KVS + t * 1024 + l * 16) + *(LAS f32x4*)(L + L_KVS + (t + 4) * 1024 + l * 16);
    v4u f0 = {pack2(T[0].x, T[0].y), pack2(T[0].z, T[0].w), pack2(T[2].x, T[2].y), pack2(T[2].z, T[2].w)};
    v4u f1 = {pack2(T[1].x, T[1].y), pack2(T[1].z, T[1].w), pack2(T[3].x, T[3].y), pack2(T[3].z, T[3].w)};
    v4u fz = {pack2(Z[0].x, Z[0].y), pack2(Z[0].z, Z[0].w), pack2(Z[1].x, Z[1].y), pack2(Z[1].z, Z[1].w)};
    const f32x4 z4 = {0.f, 0.f, 0.f, 0.f};
    f32x4 den = mfma(fz, pqf, z4), n0 = mfma(f0, pqf, z4), n1 = mfma(f1, pqf, z4);
    const float rd = __builtin_amdgcn_rcpf(den.x + 1e-6f);
    const float rd1 = g == 0 ? 0.f : rd;
    v4u of = {pack2(n0.x * rd, n0.y * rd), pack2(n0.z * rd, n0.w * rd), pack2(n1.x * rd1, n1.y * rd1), pack2(n1.z * rd1, n1.w * rd1)};
    st_sc1(of, rW, l * 16, WS_O + (m3 * 16 + h) * 1024);
  }
  BAR_VM();
  if (tid == 0) set_flag(ws, WS_F2 + (i1 * 16 + h) * FS, tag);
  u64 earlyr = 0;
  if (w == 7) earlyr = __hip_atomic_load((const GAS u64*)(ws + WS_FR + h * 128 + c * 8), __ATOMIC_RELAXED, __HIP_MEMORY_SCOPE_AGENT);
  STAMP(8);
  asm volatile("" : "+v"(xr0), "+v"(xr1), "+v"(bb1));
  unsigned w1A = WS_R1 + ((h * 14 + r3) * 7 + w) * 1024, w1B = w1A - 14 * 7168, w1D = lbase + L_W1 + w * 1024;
  asm volatile("" : "+s"(w1A), "+s"(w1B), "+s"(w1D));
  auto w1dma = [&](int s) {
    dma16s(rW, l * 16, (r3 < 14 - s ? w1A : w1B) + s * 7168, w1D + s * 7168);
  };
  constexpr int W1A = 8;
  auto w1early = [&]() {
    if (w == 7 && !__all(earlyr == tag)) poll_flags(ws, WS_FR + h * 128 + c * 8, 0, tag);
    asm volatile("s_barrier" ::: "memory");
    if (w < 7) {
#pragma unroll
      for (int s = 0; s < W1A; ++s) w1dma(s);
    }
  };
  v4u hres;
  if (act24) {
    unsigned wb[4];
#pragma unroll
    for (int j = 0; j < 4; ++j) wb[j] = (l8 ^ (kh * 64) ^ (j * 32)) + L_WO + kh * (224 * 128);
    auto wfrag = [&](int q) {
      const int n = q >> 2, jl = q & 3;
      const unsigned po = (((n >> 1) + roc) & 3) * (56 * 128) + (n & 1) * (28 * 128);
      return trfrag2(L, wb[jl ^ ((n & 1) * 2)] + po, wb[jl ^ (((n & 1) ^ 1) * 2)] + po + 12 * 128);
    };
    unsigned oo[4];
#pragma unroll
    for (int n = 0; n < 4; ++n) {
      oo[n] = WS_O + ((4 * u + mt) * 16 + 8 * kh + 2 * ((n + roc) & 3)) * 1024;
      asm volatile("" : "+s"(oo[n]));
    }
    v4u wf[32];
#pragma unroll
    for (int q = 0; q < 32; ++q) wf[q] = wfrag(q);
    SB();
    w1early();
    const int m2 = 4 * u + mt;
    f32x4 a2[4] = {f32x4{bflo(xr0.x), bfhi(xr0.x), bflo(xr0.y), bfhi(xr0.y)}, f32x4{bflo(xr1.x), bfhi(xr1.x), bflo(xr1.y), bfhi(xr1.y)},
                   f32x4{0.f, 0.f, 0.f, 0.f}, f32x4{0.f, 0.f, 0.f, 0.f}};
    asm volatile("" : "+v"(a2[0]), "+v"(a2[1]));
    if (w == 7) {
      const GAS u64* q = (const GAS u64*)(ws + WS_F2 + ((u >> 1) * 16 + c) * FS);
      const unsigned tl = (unsigned)tag, th = (unsigned)(tag >> 32);
#pragma unroll 1
      for (unsigned it = 0; it < SPIN_CAP; ++it) {
        const u64 v = __hip_atomic_load(q, __ATOMIC_RELAXED, __HIP_MEMORY_SCOPE_AGENT);
        const v4u fa = ld_sc1(rW, l * 32, WS_FR), fb = ld_sc1(rW, l * 32 + 16, WS_FR);
        if (__all(v == tag && fa.x == tl && fa.y == th && fa.z == tl && fa.w == th && fb.x == tl && fb.y == th && fb.z == tl && fb.w == th)) break;
        asm volatile("s_sleep 1" ::: "memory");
      }
    }
    BAR();
    STAMP(10);
    v4u of[8];
    auto oload = [&](int n) { of[n] = ld_sc1(rW, l * 16 + (n & 1) * 1024, oo[n >> 1]); };
#pragma unroll
    for (int n = 0; n < 8; ++n) {
      oload(n);
      SB();
    }
#pragma unroll
    for (int q = 0; q < 32; ++q) {
      if (q == 12) asm volatile("" : "+v"(of[3]), "+v"(of[4]), "+v"(of[5]), "+v"(of[6]), "+v"(of[7]));
      if (q >= 12 && q % 3 == 0 && W1A + (q - 12) / 3 < 14 && w < 7) w1dma(W1A + (q - 12) / 3);
      a2[q & 3] = mfma(wf[q], of[q >> 2], a2[q & 3]);
      SB();
    }
    *(LAS f32x4*)(L + L_EX + w * 2048 + l * 16) = a2[2];
    *(LAS f32x4*)(L + L_EX + w * 2048 + 1024 + l * 16) = a2[3];
    BAR();
    STAMP(11);
    a2[0] += *(LAS f32x4*)(L + L_EX + (w ^ 4) * 2048 + l * 16);
    a2[1] += *(LAS f32x4*)(L + L_EX + (w ^ 4) * 2048 + 1024 + l * 16);
    hres = v4u{pack2(a2[0].x, a2[0].y), pack2(a2[0].z, a2[0].w), pack2(a2[1].x, a2[1].y), pack2(a2[1].z, a2[1].w)};
    st_sc1(hres, rW, l * 16, WS_H + (m2 * 16 + 2 * ct + kh) * 1024);
    const f32x4 z4 = {0.f, 0.f, 0.f, 0.f};
    const f32x4 gm = mfma(hres, hres, z4);
    const float t1 = mfma(on4, hres, z4)[0];
    const float t2 = (c & 2) ? ((c & 1) ? gm[3] : gm[2]) : ((c & 1) ? gm[1] : gm[0]);
    if (g == (c >> 2)) st_sc1_64(v2u{asu(t1), asu(t2)}, rW, c * 8, WS_ST + (m2 * 16 + 2 * ct + kh) * 128);
    BAR_VM();
    if (tid == 0) {
      set_flag(ws, WS_F3 + (u * 8 + ct) * FS, tag);
      if (b == 0) set_flag(ws, WS_EPOCH, tag);
    }
  } else {
    w1early();
    if (w < 7) {
#pragma unroll
      for (int s = W1A; s < 14; ++s) w1dma(s);
    }
    BAR_VM();
  }
  STAMP(12);
  v4u wf2[7][4];
  unsigned w2o[7];
#pragma unroll
  for (int kk = 0; kk < 7; ++kk) {
    w2o[kk] = WS_R2 + (ct * 56 + 7 * w + wrap(kk + r4, 7)) * 4096;
    asm volatile("" : "+s"(w2o[kk]));
  }
  auto w2load = [&](int i) { wf2[i >> 2][i & 3] = ld_sc1(rW, l * 16 + (i & 3) * 1024, w2o[i >> 2]); };
  unsigned lP3 = L_W1 + l * 16, lP3b = lP3 + 7 * 7168;
  OPQ(lP3b);
  {
    v4u f[14];
#pragma unroll
    for (int s = 0; s < 14; ++s) f[s] = *(LAS v4u*)(L + (s < 7 ? lP3 + s * 7168 : lP3b + (s - 7) * 7168) + wc * 1024);
    SB();
    f32x4 cs = {0.f, 0.f, 0.f, 0.f};
#pragma unroll
    for (int s = 0; s < 14; ++s) {
      cs = mfma(f[s], on4, cs);
      if (s < W2_A) w2load(s);
      SB();
    }
    if (c == 0) {
      *(LAS f32x4*)(L + L_C1 + wc * 64 + g * 16) = cs * GC;
      *(LAS f32x4*)(L + L_C1 + 448 + wc * 64 + g * 16) = f32x4{bflo(bb1.x), bfhi(bb1.x), bflo(bb1.y), bfhi(bb1.y)} * GC;
    }
  }
  {
    auto fs = [](int q) { return q < 56 ? q >> 2 : (q - 56) % 14; };
    auto fj = [](int q) { return q < 56 ? q & 3 : 4 + (q - 56) / 14; };
    auto frag = [&](int q) {
      const int s = fs(q), j = fj(q);
      return *(LAS v4u*)(L + (s < 7 ? lP3 + s * 7168 : lP3b + (s - 7) * 7168) + j * 1024);
    };
    constexpr int PF3 = 6;
    v4u f[98];
#pragma unroll
    for (int q = 0; q < PF3; ++q) f[q] = frag(q);
    unsigned ho[14];
#pragma unroll
    for (int s = 0; s < 14; ++s) {
      ho[s] = WS_H + (m3 * 16 + wrap(s + r3, 14)) * 1024;
      asm volatile("" : "+s"(ho[s]));
    }
    if (w == 7) poll_flags(ws, WS_F3 + i1 * 16 * FS, (c & 7) == 7 ? 0 : c, tag);
    BAR();
    STAMP(14);
    v4u hf[14];
    auto hld = [&](int s) { hf[s] = ld_sc1(rW, l * 16, ho[s]); };
    constexpr int HA = HAV;
#pragma unroll
    for (int s = 0; s < HA; ++s) {
      hld(s);
      SB();
    }
    v2u st[4];
    f32x4 a3[7];
#pragma unroll
    for (int j = 0; j < 7; ++j) a3[j] = f32x4{0.f, 0.f, 0.f, 0.f};
#pragma unroll
    for (int q = 0; q < 56; ++q) {
      f[q + PF3] = frag(q + PF3);
      a3[fj(q)] = mfma(f[q], hf[fs(q)], a3[fj(q)]);
      if (q % 4 == 0 && q / 4 + HA < 14) hld(q / 4 + HA);
      if (q >= 8 && q < 12) st[q - 8] = ld_sc1_64(rW, c * 8 + g * 128 + (q - 8) * 512, WS_ST + m3 * 2048);
      if (q >= 32 && q % 4 == 0) w2load(W2_A + (q - 32) / 4);
      SB();
    }
    STAMP(20);
    float t1 = asf(st[0].x) + asf(st[1].x) + asf(st[2].x) + (g < 2 ? asf(st[3].x) : 0.f);
    float t2 = asf(st[0].y) + asf(st[1].y) + asf(st[2].y) + (g < 2 ? asf(st[3].y) : 0.f);
    const float mean2 = colsum(t1) * (1.0f / 448.0f);
    const float rs2 = __builtin_amdgcn_rsqf(fmaxf(__builtin_fmaf(colsum(t2), 1.0f / 448.0f, -mean2 * mean2), 0.f) + 1e-5f);
    const float nm2 = -mean2 * rs2, rs2c = rs2 * GC;
    const unsigned ksp = (7 * h + 1) >> 1;
    const unsigned ob = WS_H1 + (m3 * 56 + ksp) * 1024;
    unsigned pk[14];
    auto epi1 = [&](int j, int r, const f32x4& cs, const f32x4& bb) {
      return gelu_s(__builtin_fmaf(a3[j][r], rs2c, __builtin_fmaf(nm2, cs[r], bb[r])));
    };
#pragma unroll
    for (int jj = 0; jj < 7; ++jj) {
      f32x4 cs = *(LAS f32x4*)(L + L_C1 + jj * 64 + g * 16);
      f32x4 bb = *(LAS f32x4*)(L + L_C1 + 448 + jj * 64 + g * 16);
      SB();
      float y[4];
#pragma unroll
      for (int s = 0; s < 7; ++s) {
        const int q = 56 + jj * 7 + s;
        if (jj < 6) {
          if (q + PF3 < 98) f[q + PF3] = frag(q + PF3);
          a3[fj(q)] = mfma(f[q], hf[fs(q)], a3[fj(q)]);
          if ((s == 1 || s == 4) && 6 + jj * 2 + s / 4 < W2_B) w2load(W2_A + 6 + jj * 2 + s / 4);
        }
        if (s == 2 || s == 4) {
          y[s - 2] = epi1(jj, s - 2, cs, bb);
          y[s - 1] = epi1(jj, s - 1, cs, bb);
        }
        if (s == 3 || s == 5) pk[2 * jj + (s - 3) / 2] = pack2(y[s - 3], y[s - 2]);
        if (s != 2 && s != 4) SB();
      }
      if (jj & 1) st_sc1(v4u{pk[2 * jj - 2], pk[2 * jj - 1], pk[2 * jj], pk[2 * jj + 1]}, rW, l * 16 + (jj >> 1) * 1024, ob);
    }
    st_sc1_64(v2u{pk[12], pk[13]}, rW, l * 16, (h & 1) ? ob - 1016 : ob + 3072);
    STAMP(21);
    BAR_VM();
    if (tid == 0) set_flag(ws, WS_F4 + (i1 * 16 + h) * FS, tag);
  }
  STAMP(15);
  if (act24) {
#pragma unroll
    for (int i = W2_A + W2_B; i < 28; ++i) w2load(i);
    v4u bb2 = *(const GAS v4u*)(b2 + 64 * ct + (tid & 7) * 8);
    *(LAS v2u*)(L + L_HT + (16 * mt + c) * 144 + (32 * kh + 4 * g) * 2) = v2u{hres.x, hres.y};
    *(LAS v2u*)(L + L_HT + (16 * mt + c) * 144 + (32 * kh + 16 + 4 * g) * 2) = v2u{hres.z, hres.w};
    unsigned ko[7], mo[4];
#pragma unroll
    for (int kk = 0; kk < 7; ++kk) {
      ko[kk] = l * 16 + wrap(kk + r4, 7) * 1024;
      asm volatile("" : "+v"(ko[kk]));
    }
#pragma unroll
    for (int m = 0; m < 4; ++m) {
      mo[m] = WS_H1 + ((4 * u + ((m + rmc) & 3)) * 56 + 7 * w) * 1024;
      asm volatile("" : "+s"(mo[m]));
    }
    if (w == 7) poll_flags(ws, WS_F4 + (u >> 1) * 16 * FS, c, tag);
    BAR();
    STAMP(17);
    v4u hf[28];
    auto hload = [&](int i) {
      const int m = i / 7, kk = i % 7;
      hf[i] = ld_sc1(rW, ko[kk], mo[m]);
    };
    constexpr int HB = HBV;
#pragma unroll
    for (int i = 0; i < HB; ++i) {
      hload(i);
      SB();
    }
    f32x4 a4[4][4];
    auto ptw = [&](int m, int j) { *(LAS f32x4*)(L + L_PT + w * 17408 + ((m + rmc) & 3) * (16 * 272) + c * 272 + g * 16 + j * 64) = a4[m][j]; };
#pragma unroll
    for (int m = 0; m < 4; ++m) {
#pragma unroll
      for (int j = 0; j < 4; ++j) a4[m][j] = f32x4{0.f, 0.f, 0.f, 0.f};
#pragma unroll
      for (int kk = 0; kk < 7; ++kk) {
        if (7 * m + kk + HB < 28) hload(7 * m + kk + HB);
        if (m > 0 && kk >= 1 && kk <= 4) ptw(m - 1, kk - 1);
#pragma unroll
        for (int j = 0; j < 4; ++j) a4[m][j] = mfma(wf2[kk][j], hf[7 * m + kk], a4[m][j]);
        SB();
      }
    }
#pragma unroll
    for (int j = 0; j < 4; ++j) ptw(3, j);
    BAR();
    STAMP(18);
    const unsigned pa = L_PT + (tid >> 3) * 272 + (tid & 7) * 32;
    f32x4 o0 = *(LAS f32x4*)(L + pa), o1 = *(LAS f32x4*)(L + pa + 16);
#pragma unroll
    for (int ww = 1; ww < 8; ++ww) {
      o0 += *(LAS f32x4*)(L + pa + ww * 17408);
      o1 += *(LAS f32x4*)(L + pa + ww * 17408 + 16);
    }
    v4u hh = *(LAS v4u*)(L + L_HT + (tid >> 3) * 144 + (tid & 7) * 16);
    constexpr float IG = 1.0f / GC;
    v4u ov = {pack2(o0.x * IG + (bflo(bb2.x) + bflo(hh.x)), o0.y * IG + (bfhi(bb2.x) + bfhi(hh.x))),
              pack2(o0.z * IG + (bflo(bb2.y) + bflo(hh.y)), o0.w * IG + (bfhi(bb2.y) + bfhi(hh.y))),
              pack2(o1.x * IG + (bflo(bb2.z) + bflo(hh.z)), o1.y * IG + (bfhi(bb2.z) + bfhi(hh.z))),
              pack2(o1.z * IG + (bflo(bb2.w) + bflo(hh.w)), o1.w * IG + (bfhi(bb2.w) + bfhi(hh.w)))};
    *(GAS v4u*)(out + (64 * u + (tid >> 3)) * 448 + 64 * ct + (tid & 7) * 8) = ov;
  }
  STAMP(19);
  asm volatile(".Lfb_end:" ::: "memory");
}

extern "C" void kernel_launch(void* const* d_in, const int* in_sizes, int n_in, void* d_out, int out_size, void* d_ws,
                              size_t ws_size, hipStream_t stream) {
  (void)in_sizes; (void)n_in; (void)out_size; (void)ws_size;
  fused_block<<<256, 512, 0, stream>>>((const u16*)d_in[0], (const u16*)d_in[1], (const u16*)d_in[2], (const u16*)d_in[4],
                                       (const u16*)d_in[5], (const u16*)d_in[6], (const u16*)d_in[7], (u16*)d_out,
                                       (unsigned char*)d_ws);
}
```

```cpp
#include <hip/hip_runtime.h>
#include <hip/hip_bf16.h>

#define LAS __attribute__((address_space(3)))
#define GAS __attribute__((address_space(1)))
#define DI __device__ __forceinline__
typedef unsigned v4u __attribute__((ext_vector_type(4)));
typedef unsigned v2u __attribute__((ext_vector_type(2)));
typedef float f32x4 __attribute__((ext_vector_type(4)));
typedef float f32x2 __attribute__((ext_vector_type(2)));
typedef short s16x4 __attribute__((ext_vector_type(4)));
typedef __bf16 bf16x8 __attribute__((ext_vector_type(8)));
typedef __bf16 bf16x2 __attribute__((ext_vector_type(2)));
typedef unsigned long long u64;
typedef unsigned short u16;
typedef __amdgpu_buffer_rsrc_t rsrc_t;

#define W2_A 6
#define STRAD 1
#define TAIL 0
#define FULLF 1
#define PRET 1
#define FRL 1
#define HAV 14
#define QBV 10
#define HBV 10
#define W2_B 16

namespace {
constexpr int S_ = 2048, D_ = 448, F_ = 1792;
constexpr unsigned FS = FULLF ? 128 : 64;
constexpr unsigned WS_EPOCH = 0;
constexpr unsigned WS_F1 = 4096;
constexpr unsigned WS_F2 = WS_F1 + 256 * FS;
constexpr unsigned WS_F3 = WS_F2 + 256 * FS;
constexpr unsigned WS_F4 = WS_F3 + 256 * FS;
constexpr unsigned WS_FL = WS_F4 + 256 * FS;
constexpr unsigned WS_FR = WS_FL + 256 * FS;
constexpr unsigned WS_KVP = 262144;
constexpr unsigned WS_O = WS_KVP + (1u << 20);
constexpr unsigned WS_H = WS_O + (2u << 20);
constexpr unsigned WS_ST = WS_H + (2u << 20);
constexpr unsigned WS_H1 = WS_ST + (256u << 10);
constexpr unsigned WS_R1 = WS_H1 + 128u * 56u * 1024u;
constexpr unsigned WS_R2 = WS_R1 + 16u * 14u * 7168u;
constexpr unsigned WS_END = WS_R2 + 7u * 56u * 4096u;
constexpr unsigned WS_DBG = 32u << 20;
constexpr unsigned K = 1024;
constexpr unsigned L_KV = 0, L_Q = 56 * K, L_X = 84 * K, L_WO = 100 * K, L_SC = 156 * K;
constexpr unsigned L_CSQ = L_SC, L_CS1 = L_SC + 512, L_C1 = L_SC + 1024, L_CNT = L_SC + 2048;
constexpr unsigned L_EX = 104 * K, L_KVS = 88 * K, L_RP = 0;
constexpr unsigned L_W1 = 0, L_PT = 0, L_HT = 140 * K;
constexpr unsigned LDS_BYTES = 163840;
constexpr int AUX_SC1 = 16;
constexpr unsigned SPIN_CAP = 1u << 18;
constexpr unsigned xslot(int i) { return 88 * K + (i & 3) * 16 * K; }

DI f32x4 mfma(v4u a, v4u b, f32x4 c) {
  return __builtin_amdgcn_mfma_f32_16x16x32_bf16(__builtin_bit_cast(bf16x8, a), __builtin_bit_cast(bf16x8, b), c, 0, 0, 0);
}
DI f32x4 rowsum(float v) {
  asm volatile("s_nop 2" : "+v"(v));
  return __builtin_amdgcn_mfma_f32_16x16x4f32(v, 1.0f, f32x4{0.f, 0.f, 0.f, 0.f}, 0, 0, 0);
}
DI float colsum(float v) {
  asm volatile("s_nop 2" : "+v"(v));
  return __builtin_amdgcn_mfma_f32_16x16x4f32(1.0f, v, f32x4{0.f, 0.f, 0.f, 0.f}, 0, 0, 0)[0];
}
DI unsigned pack2(float a, float b) {
  bf16x2 v = {(__bf16)a, (__bf16)b};
  return __builtin_bit_cast(unsigned, v);
}
DI float asf(unsigned u) { return __builtin_bit_cast(float, u); }
DI unsigned asu(float f) { return __builtin_bit_cast(unsigned, f); }
DI float bflo(unsigned u) { return asf(u << 16); }
DI float bfhi(unsigned u) { return asf(u & 0xffff0000u); }
DI float dot2(unsigned a, unsigned b, float c) {
  return __builtin_amdgcn_fdot2_f32_bf16(__builtin_bit_cast(bf16x2, a), __builtin_bit_cast(bf16x2, b), c, false);
}
DI v4u trfrag2(LAS unsigned char* L, unsigned a, unsigned b) {
  s16x4 lo = __builtin_amdgcn_ds_read_tr16_b64_v4i16((LAS s16x4*)(L + a));
  s16x4 hi = __builtin_amdgcn_ds_read_tr16_b64_v4i16((LAS s16x4*)(L + b));
  v2u l2 = __builtin_bit_cast(v2u, lo), h2 = __builtin_bit_cast(v2u, hi);
  return v4u{l2.x, l2.y, h2.x, h2.y};
}
DI v4u trfrag(LAS unsigned char* L, unsigned a, unsigned dt) { return trfrag2(L, a, a + dt); }
DI void dma16(rsrc_t rs, unsigned voff, unsigned soff, unsigned ldst) {
  asm volatile("s_mov_b32 m0, %0\n\ts_nop 0\n\tbuffer_load_dwordx4 %1, %2, %3 offen lds" ::"s"(ldst), "v"(voff), "s"(rs), "s"(soff) : "memory");
}
DI void dma16s(rsrc_t rs, unsigned voff, unsigned soff, unsigned ldst) {
  asm volatile("s_mov_b32 m0, %0\n\ts_nop 0\n\tbuffer_load_dwordx4 %1, %2, %3 offen sc1 lds" ::"s"(ldst), "v"(voff), "s"(rs), "s"(soff) : "memory");
}
DI void dma16x2(rsrc_t rs, unsigned voff, unsigned voff2, unsigned soff, unsigned ldst) {
  asm volatile("s_mov_b32 m0, %0\n\ts_nop 0\n\tbuffer_load_dwordx4 %1, %3, %4 offen lds\n\tbuffer_load_dwordx4 %2, %3, %4 offen offset:1024 lds"
               ::"s"(ldst), "v"(voff), "v"(voff2), "s"(rs), "s"(soff) : "memory");
}
template <int STR> DI void dma16x4(rsrc_t rs, unsigned voff, unsigned soff, unsigned ldst) {
  unsigned t;
  asm volatile("s_mov_b32 m0, %1\n\ts_add_u32 %0, %4, %5\n\tbuffer_load_dwordx4 %2, %3, %4 offen lds\n\t"
               "buffer_load_dwordx4 %2, %3, %0 offen offset:1024 lds\n\ts_add_u32 %0, %0, %5\n\t"
               "buffer_load_dwordx4 %2, %3, %0 offen offset:2048 lds\n\ts_add_u32 %0, %0, %5\n\t"
               "buffer_load_dwordx4 %2, %3, %0 offen offset:3072 lds"
               : "=&s"(t) : "s"(ldst), "v"(voff), "s"(rs), "s"(soff), "i"(STR - 1024) : "memory", "scc");
}
#define BAR() asm volatile("s_waitcnt lgkmcnt(0)\n\ts_barrier" ::: "memory")
#define BAR_VM() asm volatile("s_waitcnt vmcnt(0) lgkmcnt(0)\n\ts_barrier" ::: "memory")
#define DRAIN() asm volatile("s_waitcnt vmcnt(0)" ::: "memory")
#define SB() __builtin_amdgcn_sched_barrier(0)
#define OPQ(x) asm("" : "+v"(x))
constexpr float LOG2E = 1.4426950408889634f;
DI float phi(float t, float rs, float rl) { return __builtin_amdgcn_exp2f(fminf(t * rl, 0.f)) + fmaxf(__builtin_fmaf(t, rs, 1e-6f), 1e-6f); }
constexpr float GC = 0.4686686930532063f, GK = -4.91222953926424f;
DI float gelu_s(float v) {
  float e = __builtin_amdgcn_exp2f(v * __builtin_fmaf(-v, v, GK));
  float y = v * __builtin_amdgcn_rcpf(1.0f + e);
  OPQ(y);
  return y;
}
DI void poll_flags(unsigned char* ws, unsigned off, int idx, u64 tag) {
  const GAS u64* q = (const GAS u64*)(ws + off + idx * FS);
#pragma unroll 1
  for (unsigned it = 0; it < SPIN_CAP; ++it) {
    u64 v = __hip_atomic_load(q, __ATOMIC_RELAXED, __HIP_MEMORY_SCOPE_AGENT);
    if (__all(v == tag)) break;
    __builtin_amdgcn_s_sleep(1);
  }
  __builtin_amdgcn_fence(__ATOMIC_ACQUIRE, "wavefront");
}
DI void set_flag(unsigned char* ws, unsigned off, u64 tag) {
  __hip_atomic_store((GAS u64*)(ws + off), tag, __ATOMIC_RELAXED, __HIP_MEMORY_SCOPE_AGENT);
}
DI void set_flag_line(unsigned char* ws, unsigned off, u64 tag, int t) {
  if (t < (FULLF ? 16 : 1)) set_flag(ws, off + t * 8, tag);
}
DI v4u ld_sc1(rsrc_t rs, unsigned voff, unsigned soff) { return __builtin_amdgcn_raw_buffer_load_b128(rs, voff, soff, AUX_SC1); }
DI v2u ld_sc1_64(rsrc_t rs, unsigned voff, unsigned soff) { return __builtin_amdgcn_raw_buffer_load_b64(rs, voff, soff, AUX_SC1); }
DI void st_sc1(v4u v, rsrc_t rs, unsigned voff, unsigned soff) { __builtin_amdgcn_raw_buffer_store_b128(v, rs, voff, soff, AUX_SC1); }
DI void st_sc1_64(v2u v, rsrc_t rs, unsigned voff, unsigned soff) { __builtin_amdgcn_raw_buffer_store_b64(v, rs, voff, soff, AUX_SC1); }
DI int wrap(int v, int m) { return v >= m ? v - m : v; }

#define STAMP(k) do { } while (0)
}

__global__ void __launch_bounds__(512, 2)
fused_block(const u16* __restrict__ x, const u16* __restrict__ wqkv, const u16* __restrict__ wo, const u16* __restrict__ w1,
            const u16* __restrict__ b1, const u16* __restrict__ w2, const u16* __restrict__ b2, u16* __restrict__ out,
            unsigned char* ws) {
  __shared__ __attribute__((aligned(1024))) unsigned char smem[LDS_BYTES];
  LAS unsigned char* L = (LAS unsigned char*)smem;
  const unsigned lbase = (unsigned)(size_t)L;
  STAMP(0);
  const int tid = threadIdx.x, l = tid & 63, w = __builtin_amdgcn_readfirstlane(tid >> 6), g = l >> 4, c = l & 15;
  const int b = blockIdx.x, xcd = b & 7, slot = b >> 3;
  const int i1 = 4 * (xcd >> 1) + (slot >> 3), h = 8 * (xcd & 1) + (slot & 7);
  const int u = 4 * xcd + (slot >> 3), ct = slot & 7;
  const bool act24 = ct < 7;
  int m3 = 8 * i1 + w;
  asm volatile("" : "+s"(m3));
  const int sa = slot >> 3;
#define ROT 64
#define R1M 0
  const int r1 = R1M == 0 ? slot % 7 : R1M == 1 ? 0 : R1M == 2 ? 2 * sa : R1M == 3 ? (ct == 7 ? 0 : ct) : R1M == 4 ? (sa & 1) * 3 : R1M == 5 ? (ct & 1) * 3 : (ct >> 1) * 2, r3 = (ROT & 2) ? wrap(4 * sa + 2 * ct, 14) : 0, r4 = (ROT & 4) ? (2 * sa + ct) % 7 : 0;
  const int rka = (ROT & 8) ? sa : 0, roc = (ROT & 16) ? ct : 0, rmc = (ROT & 32) ? ct : 0, rwo = (ROT & 64) ? sa : 0;

  const rsrc_t rX = __builtin_amdgcn_make_buffer_rsrc((void*)x, 0, S_ * D_ * 2, 0x00020000);
  const rsrc_t rQ = __builtin_amdgcn_make_buffer_rsrc((void*)wqkv, 0, D_ * 3 * D_ * 2, 0x00020000);
  const rsrc_t rO = __builtin_amdgcn_make_buffer_rsrc((void*)wo, 0, D_ * D_ * 2, 0x00020000);
  const rsrc_t rA = __builtin_amdgcn_make_buffer_rsrc((void*)w1, 0, D_ * F_ * 2, 0x00020000);
  const rsrc_t rB = __builtin_amdgcn_make_buffer_rsrc((void*)w2, 0, F_ * D_ * 2, 0x00020000);
  const rsrc_t rW = __builtin_amdgcn_make_buffer_rsrc((void*)ws, 0, WS_END, 0x00020000);

  const unsigned vW = (l >> 2) * 2688 + (((l >> 1) & 1) ^ (l >> 5)) * 24 + (l & 1) * 16;
  const unsigned vXa = (16 * m3 + (l >> 3)) * 896 + ((l & 7) ^ g) * 16;
  const unsigned vXb = (vXa ^ 64) + 8 * 896;
  const unsigned sW = (32 * (w >> 2) + 16 * (w & 1)) * 2688 + (1 + ((w >> 1) & 1)) * 896 + 56 * h;
  auto issue_w = [&](int i) { dma16(rQ, vW, sW + wrap(i + r1, 7) * 64 * 2688, lbase + L_KV + w * 1024 + i * 8192); };
  auto issue_xa = [&](int i) { dma16(rX, vXa, wrap(i + r1, 7) * 128, lbase + xslot(i) + w * 2048); };
  auto issue_xb = [&](int i) { dma16(rX, vXb, wrap(i + r1, 7) * 128, lbase + xslot(i) + w * 2048 + 1024); };
  auto issue_q = [&](int m) {
    int n = w + 8 * m;
    if (m == 3) n = n >= 28 ? n - 4 : n;
    dma16(rQ, vW, (64 * wrap((n >> 2) + r1, 7) + 16 * (n & 3)) * 2688 + 56 * h, lbase + L_Q + n * 1024);
  };
  auto stage_issue = [&](int i) {
    issue_w(i);
    issue_xa(i);
    issue_xb(i);
  };
  stage_issue(0);
  const unsigned v8 = (l >> 3) * 896 + (((l >> 1) & 3) ^ g) * 32 + (l & 1) * 16 + ct * 128;
  unsigned v1off;
  {
    const int ch = 64 * (w < 7 ? w : 6) + l, row = (ch * 2341) >> 15, cc = ch - 14 * row, js = cc >> 1;
    const int j = (h & 1) ? (js == 6 ? 0 : js + 1) : js;
    v1off = row * 3584 + (112 * h + 16 * j) * 2 + (cc & 1) * 16;
  }
  const int wc = w < 7 ? w : 6;
  const int mt = w & 3, kh = w >> 2;
  unsigned l8 = (l >> 5) * 1024 + ((g & 1) * 4 + (c >> 2)) * 128 + ((g & 1) * 2 + (c >> 3)) * 32 + (c & 3) * 8;
  const int rk1 = i1 < 14 ? i1 : 13, rb2 = w < 4 || b >= 136 ? b : b + 256;
  v4u r2f;
  unsigned cpf = 0;
  v2u ep = {0u, 0u}, ep2 = ep;
  auto aux_loads = [&]() {
    u64 pc;
    unsigned sz;
    asm volatile(".Lfb_start:\n\ts_getpc_b64 %0\n\ts_mov_b32 %1, .Lfb_end-.Lfb_start" : "=s"(pc), "=s"(sz));
    const rsrc_t rC = __builtin_amdgcn_make_buffer_rsrc((void*)pc, 0, sz, 0x00020000);
    asm volatile("s_nop 4\n\tbuffer_load_dword %0, %1, %2, 0 offen" : "=v"(cpf) : "v"(w == 7 ? ((slot & 3) * 64 + l) * 128 : 0x7FFFFFF0), "s"(rC) : "memory");
    const int sel = (g & 1) ? (u >> 1) : (g & 2) ? i1 : h;
    const unsigned f6 = WS_F1 + (g * 256 + sel * 16 + c) * FS, f5 = WS_FL + ((g ? 256 : h * 16) + c) * FS;
    const unsigned fo = w == 6 ? f6 : f5;
    asm volatile("buffer_load_dwordx2 %0, %1, %2, 0 offen sc1" : "=v"(ep) : "v"(PRET && (w == 5 || w == 6) ? fo : 0u), "s"(rW) : "memory");
    asm volatile("buffer_load_dwordx2 %0, off, %1, 0 sc1" : "=v"(ep2) : "s"(rW) : "memory");
  };
  STAMP(1);
  const unsigned lq0 = (l >> 5) * 1024 + (g & 1) * 544 + (c >> 2) * 64 + (c & 3) * 8, lq1 = lq0 ^ 32;
  unsigned lx0 = xslot(0) + w * 2048 + c * 128 + (g ^ (c >> 1)) * 16, lx1 = lx0 ^ 64;
  OPQ(lx0);
  OPQ(lx1);
  const unsigned xw = L_X + (w >> 1) * 1024 + l * 16 + (w & 1) * 8;
  v4u xf[14];
  f32x4 acc[4], as1 = {0.f, 0.f, 0.f, 0.f}, as2 = as1, acs = as1;
  const unsigned csa = (lq0 ^ ((w & 1) * 32)) + L_KV + (w >> 2) * 4096 + ((w >> 1) & 1) * 2048;
#pragma unroll
  for (int t = 0; t < 4; ++t) acc[t] = f32x4{0.f, 0.f, 0.f, 0.f};
  const unsigned ONES = 0x3F803F80u;
  const v4u on4 = {ONES, ONES, ONES, ONES};
  u64 tag = 0;
  float rs[4], mn[4];
  if (tid == 0) *(LAS unsigned*)(L + L_CNT) = 0u;
  v4u f[2][8], fc[2];
  auto mm = [&](int i, int n) {
    const int ss = n / 6, r = n % 6;
    if (n == 12) acs = mfma(on4, fc[i & 1], acs);
    else if (r == 0) as1 = mfma(xf[2 * i + ss], on4, as1);
    else if (r == 1) as2 = mfma(xf[2 * i + ss], xf[2 * i + ss], as2);
    else acc[r - 2] = mfma(xf[2 * i + ss], f[i & 1][4 * ss + r - 2], acc[r - 2]);
  };
  auto rd = [&](int i, int n) {
    if (n == 0) xf[2 * i] = *(LAS v4u*)(L + (i & 3) * 16 * K + lx0);
    else if (n == 5) xf[2 * i + 1] = *(LAS v4u*)(L + (i & 3) * 16 * K + lx1);
    else if (n == 10) fc[i & 1] = trfrag(L, csa + i * 8192, 256);
    else {
      const int q = n < 5 ? n - 1 : n - 2;
      f[i & 1][q] = trfrag(L, ((q & 1) ? lq1 : lq0) + L_KV + (2 * i + (q >> 2)) * 4096 + ((q >> 1) & 1) * 2048, 256);
    }
  };
  float cs0[4];
  v4u g2a, fc6;
#pragma unroll
  for (int i = 0; i < 7; ++i) {
    constexpr int NW[7] = {0, 10, 9, 9, 7, 6, 4};
    asm volatile("s_waitcnt vmcnt(%3) lgkmcnt(0)\n\ts_barrier" : "+v"(ep), "+v"(ep2), "+v"(cpf) : "i"(NW[i]) : "memory");
    if (i == 0) STAMP(13);
    if (i == 3) STAMP(16);
    if (i == 6) STAMP(9);
#pragma unroll
    for (int n = 0; n < 13; ++n) {
      if (n < (i < 6 ? 11 : 10)) rd(i, n);
      if (i > 0 && i < 6) mm(i - 1, n);
      if (i == 0) {
        if (n == 0) stage_issue(1);
        if (n == 1) issue_w(2);
        if (n == 2) issue_w(3);
        if (n == 3) issue_xa(2);
        if (n == 4) issue_xb(2);
        if (n == 5) issue_w(4);
        if (n == 6) issue_xa(3);
        if (n == 7) issue_xb(3);
        if (n == 8) aux_loads();
      }
      if (i == 1 || i == 2) {
        if (n == 1) issue_w(i + 4);
        if (n == 5) issue_xa(i + 3);
        if (n == 9) issue_xb(i + 3);
      }
      if (i == 3) {
        if (n == 1) issue_xa(6);
        if (n == 4) issue_xb(6);
        if (n == 7) dma16(rA, v1off, rk1 * 32 * 3584, lbase + L_RP + 8 * K + wc * 1024);
        if (n == 10) dma16(rB, v8 - ct * 128, (32 * (rb2 % 56) + 8 * (w & 3)) * 896 + (rb2 / 56) * 128, lbase + L_RP + w * 1024);
      }
      if (i == 4 || i == 5) {
        if (n == 1) issue_q(2 * i - 8);
        if (n == 7) issue_q(2 * i - 7);
      }
      if (i == 5 && n == 11) fc6 = trfrag(L, csa + 6 * 8192, 256);
      if (i == 6 && n >= 4 && n < 12) mm(6, n < 8 ? (n & 1) + 6 * ((n - 4) >> 1) : n - 6);
      SB();
    }
    if (i == 5) {
      mm(5, 12);
      acs = mfma(on4, fc6, acs);
#pragma unroll
      for (int n = 0; n < 6; ++n) mm(5, n);
      if (g == 0) *(LAS float*)(L + L_CS1 + w * 64 + c * 4) = acs[0];
#pragma unroll
      for (int n = 6; n < 12; ++n) mm(5, n);
    }
    if (i == 4) {
      const u64 e = ((u64)(unsigned)__builtin_amdgcn_readfirstlane(ep2.y) << 32) | (unsigned)__builtin_amdgcn_readfirstlane(ep2.x);
      tag = e * 6364136223846793005ull + 1442695040888963407ull;
    }
    SB();
  }
  float dg1, dg2;
  {
#pragma unroll
    for (int t = 0; t < 4; ++t) cs0[t] = *(LAS float*)(L + L_CS1 + t * 64 + c * 4) + *(LAS float*)(L + L_CS1 + (t + 4) * 64 + c * 4);
    {
      const unsigned ra = ((l >> 5) * 8 + (g & 1) * 4 + (c >> 2)) * 224 + (c & 3) * 8 + L_RP + 8 * K;
      g2a = trfrag(L, ra + wc * 32, 3584);
      r2f = trfrag(L, (l8 ^ ((w & 3) * 32)) + L_RP + (w >> 2) * 4096, 2048);
    }
    const bool b0 = c & 1, b1 = c & 2, dg = g == (c >> 2);
    dg2 = b1 ? (b0 ? as2[3] : as2[2]) : (b0 ? as2[1] : as2[0]);
    dg2 = dg ? dg2 : 0.f;
    const f32x4 a2 = rowsum(dg2);
    SB();
#pragma unroll
    for (int n = 8; n < 12; ++n) mm(6, n);
#pragma unroll
    for (int r = 0; r < 4; ++r) {
      mn[r] = as1[r] * (-1.0f / 448.0f);
      rs[r] = __builtin_amdgcn_rsqf(fmaxf(__builtin_fmaf(a2[r], 1.0f / 448.0f, -mn[r] * mn[r]), 0.f) + 1e-5f);
    }
    dg1 = b1 ? (b0 ? as1[3] : as1[2]) : (b0 ? as1[1] : as1[0]);
    dg1 = dg ? dg1 : 0.f;
  }
  STAMP(2);
#pragma unroll
  for (int t = 0; t < 4; ++t) {
    float v[4];
#pragma unroll
    for (int r = 0; r < 4; ++r) {
      const float d = __builtin_fmaf(mn[r], cs0[t], acc[t][r]);
      v[r] = t < 2 ? phi(d, rs[r], rs[r] * LOG2E) : d * rs[r];
      if (t == 3) v[r] = c < 4 ? 1.0f : v[r];
      OPQ(v[r]);
    }
    *(LAS v2u*)(L + xw + t * 4096) = v2u{pack2(v[0], v[1]), pack2(v[2], v[3])};
  }
  BAR_VM();
  STAMP(3);
  if (w < 4) {
    f32x4 kv = {0.f, 0.f, 0.f, 0.f};
    v4u a[4], bb[4];
#pragma unroll
    for (int ks = 0; ks < 4; ++ks) {
      a[ks] = *(LAS v4u*)(L + L_X + (((w >> 1) * 4 + ks) * 64 + l) * 16);
      bb[ks] = *(LAS v4u*)(L + L_X + (((2 + (w & 1)) * 4 + ks) * 64 + l) * 16);
    }
#pragma unroll
    for (int ks = 0; ks < 4; ++ks) kv = mfma(a[ks], bb[ks], kv);
    if (w >= 2 && g == 0) kv = f32x4{0.f, 0.f, 0.f, 0.f};
    st_sc1(__builtin_bit_cast(v4u, kv), rW, l * 16, WS_KVP + ((h * 4 + w) * 16 + i1) * 1024);
  }
  v4u pqf;
  v2u xr0, xr1, bb1;
  {
    constexpr int QB = QBV;
    f32x4 aq[2] = {f32x4{0.f, 0.f, 0.f, 0.f}, f32x4{0.f, 0.f, 0.f, 0.f}};
    f32x4 csq = {0.f, 0.f, 0.f, 0.f};
    unsigned lqa = lq0 + L_Q, lqb = lq1 + L_Q;
    OPQ(lqa);
    OPQ(lqb);
    const unsigned qa = (lqa ^ ((w & 1) * 32)) + (w >> 1) * 2048;
    v4u f[28], fc[4];
#pragma unroll
    for (int q = 0; q < 6; ++q) f[q] = trfrag(L, ((q & 1) ? lqb : lqa) + (q >> 1) * 2048, 256);
    SB();
    const unsigned on3 = w < 4 ? ONES : 0u;
    const v4u on43 = {on3, on3, on3, on3};
    unsigned kvo[4];
#pragma unroll
    for (int n = 0; n < 4; ++n) kvo[n] = WS_KVP + ((h * 4 + (w & 3)) * 16 + 8 * (w >> 2) + 2 * ((n + rka) & 3)) * 1024;
    u64 early = 0;
#pragma unroll
    for (int q = 0; q < 28; ++q) {
      if (q + 6 < 28) f[q + 6] = trfrag(L, ((q & 1) ? lqb : lqa) + ((q + 6) >> 1) * 2048, 256);
      if (q < 4) fc[q] = trfrag(L, qa + (q < 3 ? q * 8192 : (w < 4 ? 3 * 8192 : 0)), 256);
      aq[q & 1] = mfma(f[q], xf[q >> 1], aq[q & 1]);
      if (q >= 6 && q < 10) csq = mfma(q < 9 ? on4 : on43, fc[q - 6], csq);
      if (q == QB && w < 4) {
        DRAIN();
        unsigned old = 0;
        if (l == 0) old = __hip_atomic_fetch_add((LAS unsigned*)(L + L_CNT), 1u, __ATOMIC_RELAXED, __HIP_MEMORY_SCOPE_WORKGROUP);
        if (__builtin_amdgcn_readfirstlane(old) == 3u) set_flag_line(ws, WS_F1 + (h * 16 + i1) * FS, tag, l);
        STAMP(4);
      }
      if (q % 4 == 0 && q < 20 && w >= 4 && w < 7) {
        const int gi = wrap((w - 4) * 5 + q / 4 + 4 * rwo, 14);
        if (w < 6 || q < 16) dma16x4<8 * 896>(rO, v8, gi * 32 * 896, lbase + L_WO + gi * 4096);
      }
      if (q == 18 && g == 0) *(LAS float*)(L + L_CSQ + w * 64 + c * 4) = csq[0];
      if (q == 24 && w == 7) early = __hip_atomic_load((const GAS u64*)(ws + WS_F1 + (h * 16 + c) * FS), __ATOMIC_RELAXED, __HIP_MEMORY_SCOPE_AGENT);
      SB();
    }
    auto rstore = [&]() {
      st_sc1(g2a, rW, l * 16, WS_R1 + ((h * 14 + rk1) * 7 + wc) * 1024);
      st_sc1(r2f, rW, l * 16, WS_R2 + (rb2 * 4 + (w & 3)) * 1024);
    };
    if (w < 7) rstore();
    const float mq = colsum(dg1) * (-1.0f / 448.0f);
    const float rq = __builtin_amdgcn_rsqf(fmaxf(__builtin_fmaf(colsum(dg2), 1.0f / 448.0f, -mq * mq), 0.f) + 1e-5f);
    STAMP(5);
    if (w == 7 && !__all(early == tag)) poll_flags(ws, WS_F1 + h * 16 * FS, c, tag);
    BAR();
    STAMP(6);
    v4u p[8];
#pragma unroll
    for (int n = 0; n < 8; ++n) {
      p[n] = ld_sc1(rW, l * 16 + (n & 1) * 1024, kvo[n >> 1]);
      SB();
    }
    if (w == 7) rstore();
    const unsigned vxr = (64 * u + 16 * mt + c) * 896 + (64 * ct + 32 * kh + 4 * g) * 2;
    xr0 = __builtin_amdgcn_raw_buffer_load_b64(rX, vxr, 0, 0);
    xr1 = __builtin_amdgcn_raw_buffer_load_b64(rX, vxr + 32, 0, 0);
    bb1 = *(const GAS v2u*)(b1 + 112 * h + 16 * ((h & 1) ? (wc == 6 ? 0 : wc + 1) : wc) + 4 * g);
    SB();
#pragma unroll
    for (int t = 0; t < 2; ++t) {
      f32x4 cs = *(LAS f32x4*)(L + L_CSQ + t * 64 + g * 16);
#pragma unroll
      for (int m = 1; m < 4; ++m) cs += *(LAS f32x4*)(L + L_CSQ + (t + 2 * m) * 64 + g * 16);
      float v[4];
#pragma unroll
      for (int r = 0; r < 4; ++r) v[r] = phi(__builtin_fmaf(mq, cs[r], aq[t][r]), rq, rq * LOG2E);
      pqf[2 * t] = pack2(v[0], v[1]);
      pqf[2 * t + 1] = pack2(v[2], v[3]);
    }
    asm volatile("" : "+v"(pqf));
    SB();
    f32x4 sum = __builtin_bit_cast(f32x4, p[0]);
#pragma unroll
    for (int n = 1; n < 8; ++n) sum += __builtin_bit_cast(f32x4, p[n]);
    *(LAS f32x4*)(L + L_KVS + w * 1024 + l * 16) = sum;
  }
  BAR_VM();
  set_flag_line(ws, WS_FL + (h * 16 + i1) * FS, tag, tid);
  if (tid == 64) set_flag(ws, WS_FR + (h * 16 + i1) * 8, tag);
  STAMP(7);
  {
    f32x4 T[4], Z[2];
#pragma unroll
    for (int dt = 0; dt < 2; ++dt)
      Z[dt] = *(LAS f32x4*)(L + L_KVS + (dt * 2 + 1) * 1024 + g * 256) + *(LAS f32x4*)(L + L_KVS + (dt * 2 + 5) * 1024 + g * 256);
#pragma unroll
    for (int t = 0; t < 4; ++t) T[t] = *(LAS f32x4*)(L + L_KVS + t * 1024 + l * 16) + *(LAS f32x4*)(L + L_KVS + (t + 4) * 1024 + l * 16);
    v4u f0 = {pack2(T[0].x, T[0].y), pack2(T[0].z, T[0].w), pack2(T[2].x, T[2].y), pack2(T[2].z, T[2].w)};
    v4u f1 = {pack2(T[1].x, T[1].y), pack2(T[1].z, T[1].w), pack2(T[3].x, T[3].y), pack2(T[3].z, T[3].w)};
    v4u fz = {pack2(Z[0].x, Z[0].y), pack2(Z[0].z, Z[0].w), pack2(Z[1].x, Z[1].y), pack2(Z[1].z, Z[1].w)};
    const f32x4 z4 = {0.f, 0.f, 0.f, 0.f};
    f32x4 den = mfma(fz, pqf, z4), n0 = mfma(f0, pqf, z4), n1 = mfma(f1, pqf, z4);
    const float rd = __builtin_amdgcn_rcpf(den.x + 1e-6f);
    const float rd1 = g == 0 ? 0.f : rd;
    v4u of = {pack2(n0.x * rd, n0.y * rd), pack2(n0.z * rd, n0.w * rd), pack2(n1.x * rd1, n1.y * rd1), pack2(n1.z * rd1, n1.w * rd1)};
    st_sc1(of, rW, l * 16, WS_O + (m3 * 16 + h) * 1024);
  }
  BAR_VM();
  set_flag_line(ws, WS_F2 + (i1 * 16 + h) * FS, tag, tid);
  u64 earlyr = 0;
  if (w == 7) earlyr = __hip_atomic_load((const GAS u64*)(ws + (FRL ? WS_FL + (h * 16 + c) * FS : WS_FR + h * 128 + c * 8)), __ATOMIC_RELAXED, __HIP_MEMORY_SCOPE_AGENT);
  STAMP(8);
  asm volatile("" : "+v"(xr0), "+v"(xr1), "+v"(bb1));
  unsigned w1A = WS_R1 + ((h * 14 + r3) * 7 + w) * 1024, w1B = w1A - 14 * 7168, w1D = lbase + L_W1 + w * 1024;
  asm volatile("" : "+s"(w1A), "+s"(w1B), "+s"(w1D));
  auto w1dma = [&](int s) {
    dma16s(rW, l * 16, (r3 < 14 - s ? w1A : w1B) + s * 7168, w1D + s * 7168);
  };
  constexpr int W1A = 8;
  auto w1early = [&]() {
    if (w == 7 && !__all(earlyr == tag)) poll_flags(ws, FRL ? WS_FL + (h * 16 + c) * FS : WS_FR + h * 128 + c * 8, 0, tag);
    asm volatile("s_barrier" ::: "memory");
    if (w < 7) {
#pragma unroll
      for (int s = 0; s < W1A; ++s) w1dma(s);
    }
  };
  v4u hres;
  if (act24) {
    unsigned wb[4];
#pragma unroll
    for (int j = 0; j < 4; ++j) wb[j] = (l8 ^ (kh * 64) ^ (j * 32)) + L_WO + kh * (224 * 128);
    auto wfrag = [&](int q) {
      const int n = q >> 2, jl = q & 3;
      const unsigned po = (((n >> 1) + roc) & 3) * (56 * 128) + (n & 1) * (28 * 128);
      return trfrag2(L, wb[jl ^ ((n & 1) * 2)] + po, wb[jl ^ (((n & 1) ^ 1) * 2)] + po + 12 * 128);
    };
    unsigned oo[4];
#pragma unroll
    for (int n = 0; n < 4; ++n) {
      oo[n] = WS_O + ((4 * u + mt) * 16 + 8 * kh + 2 * ((n + roc) & 3)) * 1024;
      asm volatile("" : "+s"(oo[n]));
    }
    v4u wf[32];
#pragma unroll
    for (int q = 0; q < 32; ++q) wf[q] = wfrag(q);
    SB();
    w1early();
    const int m2 = 4 * u + mt;
    f32x4 a2[4] = {f32x4{bflo(xr0.x), bfhi(xr0.x), bflo(xr0.y), bfhi(xr0.y)}, f32x4{bflo(xr1.x), bfhi(xr1.x), bflo(xr1.y), bfhi(xr1.y)},
                   f32x4{0.f, 0.f, 0.f, 0.f}, f32x4{0.f, 0.f, 0.f, 0.f}};
    asm volatile("" : "+v"(a2[0]), "+v"(a2[1]));
    if (w == 7) {
      const GAS u64* q = (const GAS u64*)(ws + WS_F2 + ((u >> 1) * 16 + c) * FS);
      const unsigned tl = (unsigned)tag, th = (unsigned)(tag >> 32);
#pragma unroll 1
      for (unsigned it = 0; it < SPIN_CAP; ++it) {
        const u64 v = __hip_atomic_load(q, __ATOMIC_RELAXED, __HIP_MEMORY_SCOPE_AGENT);
        const v4u fa = ld_sc1(rW, l * 32, WS_FR), fb = ld_sc1(rW, l * 32 + 16, WS_FR);
        if (__all(v == tag && fa.x == tl && fa.y == th && fa.z == tl && fa.w == th && fb.x == tl && fb.y == th && fb.z == tl && fb.w == th)) break;
        asm volatile("s_sleep 1" ::: "memory");
      }
    }
    BAR();
    STAMP(10);
    v4u of[8];
    auto oload = [&](int n) { of[n] = ld_sc1(rW, l * 16 + (n & 1) * 1024, oo[n >> 1]); };
#pragma unroll
    for (int n = 0; n < 8; ++n) {
      oload(n);
      SB();
    }
#pragma unroll
    for (int q = 0; q < 32; ++q) {
      if (q == 12) asm volatile("" : "+v"(of[3]), "+v"(of[4]), "+v"(of[5]), "+v"(of[6]), "+v"(of[7]));
      if (q >= 12 && q % 3 == 0 && W1A + (q - 12) / 3 < 14 && w < 7) w1dma(W1A + (q - 12) / 3);
      a2[q & 3] = mfma(wf[q], of[q >> 2], a2[q & 3]);
      SB();
    }
    *(LAS f32x4*)(L + L_EX + w * 2048 + l * 16) = a2[2];
    *(LAS f32x4*)(L + L_EX + w * 2048 + 1024 + l * 16) = a2[3];
    BAR();
    STAMP(11);
    a2[0] += *(LAS f32x4*)(L + L_EX + (w ^ 4) * 2048 + l * 16);
    a2[1] += *(LAS f32x4*)(L + L_EX + (w ^ 4) * 2048 + 1024 + l * 16);
    hres = v4u{pack2(a2[0].x, a2[0].y), pack2(a2[0].z, a2[0].w), pack2(a2[1].x, a2[1].y), pack2(a2[1].z, a2[1].w)};
    st_sc1(hres, rW, l * 16, WS_H + (m2 * 16 + 2 * ct + kh) * 1024);
    const f32x4 z4 = {0.f, 0.f, 0.f, 0.f};
    const f32x4 gm = mfma(hres, hres, z4);
    const float t1 = mfma(on4, hres, z4)[0];
    const float t2 = (c & 2) ? ((c & 1) ? gm[3] : gm[2]) : ((c & 1) ? gm[1] : gm[0]);
    if (g == (c >> 2)) st_sc1_64(v2u{asu(t1), asu(t2)}, rW, c * 8, WS_ST + (m2 * 16 + 2 * ct + kh) * 128);
    BAR_VM();
    set_flag_line(ws, WS_F3 + (u * 8 + ct) * FS, tag, tid);
    if (b == 0 && tid == 64) set_flag(ws, WS_EPOCH, tag);
  } else {
    w1early();
    if (w < 7) {
#pragma unroll
      for (int s = W1A; s < 14; ++s) w1dma(s);
    }
    BAR_VM();
  }
  STAMP(12);
  v4u wf2[7][4];
  unsigned w2o[7];
#pragma unroll
  for (int kk = 0; kk < 7; ++kk) {
    w2o[kk] = WS_R2 + (ct * 56 + 7 * w + wrap(kk + r4, 7)) * 4096;
    asm volatile("" : "+s"(w2o[kk]));
  }
  auto w2load = [&](int i) { wf2[i >> 2][i & 3] = ld_sc1(rW, l * 16 + (i & 3) * 1024, w2o[i >> 2]); };
  unsigned lP3 = L_W1 + l * 16, lP3b = lP3 + 7 * 7168;
  OPQ(lP3b);
  {
    v4u f[14];
#pragma unroll
    for (int s = 0; s < 14; ++s) f[s] = *(LAS v4u*)(L + (s < 7 ? lP3 + s * 7168 : lP3b + (s - 7) * 7168) + wc * 1024);
    SB();
    f32x4 cs = {0.f, 0.f, 0.f, 0.f};
#pragma unroll
    for (int s = 0; s < 14; ++s) {
      cs = mfma(f[s], on4, cs);
      if (s < W2_A) w2load(s);
      SB();
    }
    if (c == 0) {
      *(LAS f32x4*)(L + L_C1 + wc * 64 + g * 16) = cs * GC;
      *(LAS f32x4*)(L + L_C1 + 448 + wc * 64 + g * 16) = f32x4{bflo(bb1.x), bfhi(bb1.x), bflo(bb1.y), bfhi(bb1.y)} * GC;
    }
  }
  {
    auto fs = [](int q) { return q < 56 ? q >> 2 : (q - 56) % 14; };
    auto fj = [](int q) { return q < 56 ? q & 3 : 4 + (q - 56) / 14; };
    auto frag = [&](int q) {
      const int s = fs(q), j = fj(q);
      return *(LAS v4u*)(L + (s < 7 ? lP3 + s * 7168 : lP3b + (s - 7) * 7168) + j * 1024);
    };
    constexpr int PF3 = 6;
    v4u f[98];
#pragma unroll
    for (int q = 0; q < PF3; ++q) f[q] = frag(q);
    unsigned ho[14];
#pragma unroll
    for (int s = 0; s < 14; ++s) {
      ho[s] = WS_H + (m3 * 16 + wrap(s + r3, 14)) * 1024;
      asm volatile("" : "+s"(ho[s]));
    }
    if (w == 7) poll_flags(ws, WS_F3 + i1 * 16 * FS, (c & 7) == 7 ? 0 : c, tag);
    BAR();
    STAMP(14);
    v4u hf[14];
    auto hld = [&](int s) { hf[s] = ld_sc1(rW, l * 16, ho[s]); };
    constexpr int HA = HAV;
#pragma unroll
    for (int s = 0; s < HA; ++s) {
      hld(s);
      SB();
    }
    v2u st[4];
    f32x4 a3[7];
#pragma unroll
    for (int j = 0; j < 7; ++j) a3[j] = f32x4{0.f, 0.f, 0.f, 0.f};
#pragma unroll
    for (int q = 0; q < 56; ++q) {
      f[q + PF3] = frag(q + PF3);
      a3[fj(q)] = mfma(f[q], hf[fs(q)], a3[fj(q)]);
      if (q % 4 == 0 && q / 4 + HA < 14) hld(q / 4 + HA);
      if (q >= 8 && q < 12) st[q - 8] = ld_sc1_64(rW, c * 8 + g * 128 + (q - 8) * 512, WS_ST + m3 * 2048);
      if (q >= 32 && q % 4 == 0) w2load(W2_A + (q - 32) / 4);
      SB();
    }
    STAMP(20);
    float t1 = asf(st[0].x) + asf(st[1].x) + asf(st[2].x) + (g < 2 ? asf(st[3].x) : 0.f);
    float t2 = asf(st[0].y) + asf(st[1].y) + asf(st[2].y) + (g < 2 ? asf(st[3].y) : 0.f);
    const float mean2 = colsum(t1) * (1.0f / 448.0f);
    const float rs2 = __builtin_amdgcn_rsqf(fmaxf(__builtin_fmaf(colsum(t2), 1.0f / 448.0f, -mean2 * mean2), 0.f) + 1e-5f);
    const float nm2 = -mean2 * rs2, rs2c = rs2 * GC;
    const unsigned ksp = (7 * h + 1) >> 1;
    const unsigned ob = WS_H1 + (m3 * 56 + ksp) * 1024;
    unsigned pk[14];
    auto epi1 = [&](int j, int r, const f32x4& cs, const f32x4& bb) {
      return gelu_s(__builtin_fmaf(a3[j][r], rs2c, __builtin_fmaf(nm2, cs[r], bb[r])));
    };
#pragma unroll
    for (int jj = 0; jj < 7; ++jj) {
      f32x4 cs = *(LAS f32x4*)(L + L_C1 + jj * 64 + g * 16);
      f32x4 bb = *(LAS f32x4*)(L + L_C1 + 448 + jj * 64 + g * 16);
      SB();
      float y[4];
#pragma unroll
      for (int s = 0; s < 7; ++s) {
        const int q = 56 + jj * 7 + s;
        if (jj < 6) {
          if (q + PF3 < 98) f[q + PF3] = frag(q + PF3);
          a3[fj(q)] = mfma(f[q], hf[fs(q)], a3[fj(q)]);
          if ((s == 1 || s == 4) && 6 + jj * 2 + s / 4 < W2_B) w2load(W2_A + 6 + jj * 2 + s / 4);
        }
        if (s == 2 || s == 4) {
          y[s - 2] = epi1(jj, s - 2, cs, bb);
          y[s - 1] = epi1(jj, s - 1, cs, bb);
        }
        if (s == 3 || s == 5) pk[2 * jj + (s - 3) / 2] = pack2(y[s - 3], y[s - 2]);
        if (s != 2 && s != 4) SB();
      }
      if (jj & 1) st_sc1(v4u{pk[2 * jj - 2], pk[2 * jj - 1], pk[2 * jj], pk[2 * jj + 1]}, rW, l * 16 + (jj >> 1) * 1024, ob);
    }
    if (STRAD) st_sc1_64(v2u{pk[12], pk[13]}, rW, l * 8, (h & 1) ? ob - 512 : ob + 3072);
    else st_sc1_64(v2u{pk[12], pk[13]}, rW, l * 16, (h & 1) ? ob - 1016 : ob + 3072);
    STAMP(21);
    BAR_VM();
    set_flag_line(ws, WS_F4 + (i1 * 16 + h) * FS, tag, tid);
  }
  STAMP(15);
  if (act24) {
#pragma unroll
    for (int i = W2_A + W2_B; i < 28; ++i) w2load(i);
    v4u bb2 = *(const GAS v4u*)(b2 + 64 * ct + (tid & 7) * 8);
    const unsigned bb2s = *(const GAS unsigned*)(b2 + 64 * ct + (tid & 31) * 2);
    *(LAS v2u*)(L + L_HT + (16 * mt + c) * 144 + (32 * kh + 4 * g) * 2) = v2u{hres.x, hres.y};
    *(LAS v2u*)(L + L_HT + (16 * mt + c) * 144 + (32 * kh + 16 + 4 * g) * 2) = v2u{hres.z, hres.w};
    unsigned ko[7], mo[4];
#pragma unroll
    for (int kk = 0; kk < 7; ++kk) {
      ko[kk] = l * 16 + wrap(kk + r4, 7) * 1024;
      asm volatile("" : "+v"(ko[kk]));
    }
#pragma unroll
    for (int m = 0; m < 4; ++m) {
      mo[m] = WS_H1 + ((4 * u + ((m + rmc) & 3)) * 56 + 7 * w) * 1024;
      asm volatile("" : "+s"(mo[m]));
    }
    if (w == 7) poll_flags(ws, WS_F4 + (u >> 1) * 16 * FS, c, tag);
    BAR();
    STAMP(17);
    v4u hf[28];
    auto hload = [&](int i) {
      const int m = i / 7, kk = i % 7;
      if (STRAD && kk == 3) {
        const v2u lo = ld_sc1_64(rW, l * 8 + 3072, mo[m]), hi = ld_sc1_64(rW, l * 8 + 3584, mo[m]);
        hf[i] = v4u{lo.x, lo.y, hi.x, hi.y};
      } else {
        hf[i] = ld_sc1(rW, ko[kk], mo[m]);
      }
    };
    constexpr int HB = HBV;
#pragma unroll
    for (int i = 0; i < HB; ++i) {
      hload(i);
      SB();
    }
    f32x4 a4[4][4];
    auto ptw = [&](int m, int j) { *(LAS f32x4*)(L + L_PT + w * 17408 + ((m + rmc) & 3) * (16 * 272) + c * 272 + g * 16 + j * 64) = a4[m][j]; };
    constexpr float IG = 1.0f / GC;
    const unsigned pa = L_PT + (tid >> 3) * 272 + (tid & 7) * 32;
    f32x4 o0 = {0.f, 0.f, 0.f, 0.f}, o1 = o0;
#pragma unroll
    for (int m = 0; m < 4; ++m) {
#pragma unroll
      for (int j = 0; j < 4; ++j) a4[m][j] = f32x4{0.f, 0.f, 0.f, 0.f};
#pragma unroll
      for (int kk = 0; kk < 7; ++kk) {
        if (7 * m + kk + HB < 28) hload(7 * m + kk + HB);
        if (m > 0 && m < 3 && kk >= 1 && kk <= 4) ptw(m - 1, kk - 1);
        if (m == 3 && kk < 2) {
          ptw(2, 2 * kk);
          ptw(2, 2 * kk + 1);
        }
        if (TAIL && m == 3 && kk == 3) BAR();
        f32x4 t[4];
        if (TAIL && m == 3 && kk >= 3 && w < 6) {
#pragma unroll
          for (int e = 0; e < 2; ++e) {
            t[2 * e] = *(LAS f32x4*)(L + pa + (2 * (kk - 3) + e) * 17408);
            t[2 * e + 1] = *(LAS f32x4*)(L + pa + (2 * (kk - 3) + e) * 17408 + 16);
          }
        }
#pragma unroll
        for (int j = 0; j < 4; ++j) a4[m][j] = mfma(wf2[kk][j], hf[7 * m + kk], a4[m][j]);
        SB();
        if (TAIL && m == 3 && kk >= 3 && w < 6) {
          o0 += t[0];
          o1 += t[1];
          o0 += t[2];
          o1 += t[3];
        }
        SB();
      }
    }
#pragma unroll
    for (int j = 0; j < 4; ++j) ptw(3, j);
    auto fin = [&]() {
      v4u hh = *(LAS v4u*)(L + L_HT + (tid >> 3) * 144 + (tid & 7) * 16);
      v4u ov = {pack2(o0.x * IG + (bflo(bb2.x) + bflo(hh.x)), o0.y * IG + (bfhi(bb2.x) + bfhi(hh.x))),
                pack2(o0.z * IG + (bflo(bb2.y) + bflo(hh.y)), o0.w * IG + (bfhi(bb2.y) + bfhi(hh.y))),
                pack2(o1.x * IG + (bflo(bb2.z) + bflo(hh.z)), o1.y * IG + (bfhi(bb2.z) + bfhi(hh.z))),
                pack2(o1.z * IG + (bflo(bb2.w) + bflo(hh.w)), o1.w * IG + (bfhi(bb2.w) + bfhi(hh.w)))};
      *(GAS v4u*)(out + (64 * u + (tid >> 3)) * 448 + 64 * ct + (tid & 7) * 8) = ov;
    };
    if (TAIL) {
      const unsigned hb = *(LAS unsigned*)(L + L_HT + (48 + (tid >> 5)) * 144 + (tid & 31) * 4);
      if (w < 6) fin();
      BAR();
      STAMP(18);
      const unsigned pb = L_PT + (48 + (tid >> 5)) * 272 + (tid & 31) * 8;
      f32x2 r[8];
#pragma unroll
      for (int ww = 0; ww < 8; ++ww) r[ww] = *(LAS f32x2*)(L + pb + ww * 17408);
      const f32x2 q = ((r[0] + r[1]) + (r[2] + r[3])) + ((r[4] + r[5]) + (r[6] + r[7]));
      *(GAS unsigned*)(out + (64 * u + 48 + (tid >> 5)) * 448 + 64 * ct + (tid & 31) * 2) =
          pack2(q.x * IG + (bflo(bb2s) + bflo(hb)), q.y * IG + (bfhi(bb2s) + bfhi(hb)));
    } else {
      BAR();
      STAMP(18);
#pragma unroll
      for (int ww = 0; ww < 8; ++ww) {
        o0 += *(LAS f32x4*)(L + pa + ww * 17408);
        o1 += *(LAS f32x4*)(L + pa + ww * 17408 + 16);
      }
      fin();
    }
  }
  STAMP(19);
  asm volatile(".Lfb_end:" ::: "memory");
}

extern "C" void kernel_launch(void* const* d_in, const int* in_sizes, int n_in, void* d_out, int out_size, void* d_ws,
                              size_t ws_size, hipStream_t stream) {
  (void)in_sizes; (void)n_in; (void)out_size; (void)ws_size;
  fused_block<<<256, 512, 0, stream>>>((const u16*)d_in[0], (const u16*)d_in[1], (const u16*)d_in[2], (const u16*)d_in[4],
                                       (const u16*)d_in[5], (const u16*)d_in[6], (const u16*)d_in[7], (u16*)d_out,
                                       (unsigned char*)d_ws);
}
```

```cpp
#include <hip/hip_runtime.h>
#include <hip/hip_bf16.h>

#define LAS __attribute__((address_space(3)))
#define GAS __attribute__((address_space(1)))
#define DI __device__ __forceinline__
typedef unsigned v4u __attribute__((ext_vector_type(4)));
typedef unsigned v2u __attribute__((ext_vector_type(2)));
typedef float f32x4 __attribute__((ext_vector_type(4)));
typedef float f32x2 __attribute__((ext_vector_type(2)));
typedef short s16x4 __attribute__((ext_vector_type(4)));
typedef __bf16 bf16x8 __attribute__((ext_vector_type(8)));
typedef __bf16 bf16x2 __attribute__((ext_vector_type(2)));
typedef unsigned long long u64;
typedef unsigned short u16;
typedef __amdgpu_buffer_rsrc_t rsrc_t;

#define W2_A 6
#define STRAD 1
#define TAIL 0
#define FULLF 1
#define PRET 1
#define FRL 1
#define HAV 14
#define QBV 10
#define HBV 10
#define W2_B 16

namespace {
constexpr int S_ = 2048, D_ = 448, F_ = 1792;
constexpr unsigned FS = FULLF ? 128 : 64;
constexpr unsigned WS_EPOCH = 0;
constexpr unsigned WS_F1 = 4096;
constexpr unsigned WS_F2 = WS_F1 + 256 * FS;
constexpr unsigned WS_F3 = WS_F2 + 256 * FS;
constexpr unsigned WS_F4 = WS_F3 + 256 * FS;
constexpr unsigned WS_FL = WS_F4 + 256 * FS;
constexpr unsigned WS_FR = WS_FL + 256 * FS;
constexpr unsigned WS_KVP = 262144;
constexpr unsigned WS_O = WS_KVP + (1u << 20);
constexpr unsigned WS_H = WS_O + (2u << 20);
constexpr unsigned WS_ST = WS_H + (2u << 20);
constexpr unsigned WS_H1 = WS_ST + (256u << 10);
constexpr unsigned WS_R1 = WS_H1 + 128u * 56u * 1024u;
constexpr unsigned WS_R2 = WS_R1 + 16u * 14u * 7168u;
constexpr unsigned WS_END = WS_R2 + 7u * 56u * 4096u;
constexpr unsigned WS_DBG = 32u << 20;
constexpr unsigned K = 1024;
constexpr unsigned L_KV = 0, L_Q = 56 * K, L_X = 84 * K, L_WO = 100 * K, L_SC = 156 * K;
constexpr unsigned L_CSQ = L_SC, L_CS1 = L_SC + 512, L_C1 = L_SC + 1024, L_CNT = L_SC + 2048;
constexpr unsigned L_EX = 104 * K, L_KVS = 88 * K, L_RP = 0;
constexpr unsigned L_W1 = 0, L_PT = 0, L_HT = 140 * K;
constexpr unsigned LDS_BYTES = 163840;
constexpr int AUX_SC1 = 16;
constexpr unsigned SPIN_CAP = 1u << 18;
constexpr unsigned xslot(int i) { return 88 * K + (i & 3) * 16 * K; }

DI f32x4 mfma(v4u a, v4u b, f32x4 c) {
  return __builtin_amdgcn_mfma_f32_16x16x32_bf16(__builtin_bit_cast(bf16x8, a), __builtin_bit_cast(bf16x8, b), c, 0, 0, 0);
}
DI f32x4 rowsum(float v) {
  asm volatile("s_nop 2" : "+v"(v));
  return __builtin_amdgcn_mfma_f32_16x16x4f32(v, 1.0f, f32x4{0.f, 0.f, 0.f, 0.f}, 0, 0, 0);
}
DI float colsum(float v) {
  asm volatile("s_nop 2" : "+v"(v));
  return __builtin_amdgcn_mfma_f32_16x16x4f32(1.0f, v, f32x4{0.f, 0.f, 0.f, 0.f}, 0, 0, 0)[0];
}
DI unsigned pack2(float a, float b) {
  bf16x2 v = {(__bf16)a, (__bf16)b};
  return __builtin_bit_cast(unsigned, v);
}
DI float asf(unsigned u) { return __builtin_bit_cast(float, u); }
DI unsigned asu(float f) { return __builtin_bit_cast(unsigned, f); }
DI float bflo(unsigned u) { return asf(u << 16); }
DI float bfhi(unsigned u) { return asf(u & 0xffff0000u); }
DI float dot2(unsigned a, unsigned b, float c) {
  return __builtin_amdgcn_fdot2_f32_bf16(__builtin_bit_cast(bf16x2, a), __builtin_bit_cast(bf16x2, b), c, false);
}
DI v4u trfrag2(LAS unsigned char* L, unsigned a, unsigned b) {
  s16x4 lo = __builtin_amdgcn_ds_read_tr16_b64_v4i16((LAS s16x4*)(L + a));
  s16x4 hi = __builtin_amdgcn_ds_read_tr16_b64_v4i16((LAS s16x4*)(L + b));
  v2u l2 = __builtin_bit_cast(v2u, lo), h2 = __builtin_bit_cast(v2u, hi);
  return v4u{l2.x, l2.y, h2.x, h2.y};
}
DI v4u trfrag(LAS unsigned char* L, unsigned a, unsigned dt) { return trfrag2(L, a, a + dt); }
DI void dma16(rsrc_t rs, unsigned voff, unsigned soff, unsigned ldst) {
  asm volatile("s_mov_b32 m0, %0\n\ts_nop 0\n\tbuffer_load_dwordx4 %1, %2, %3 offen lds" ::"s"(ldst), "v"(voff), "s"(rs), "s"(soff) : "memory");
}
DI void dma16s(rsrc_t rs, unsigned voff, unsigned soff, unsigned ldst) {
  asm volatile("s_mov_b32 m0, %0\n\ts_nop 0\n\tbuffer_load_dwordx4 %1, %2, %3 offen sc1 lds" ::"s"(ldst), "v"(voff), "s"(rs), "s"(soff) : "memory");
}
DI void dma16x2(rsrc_t rs, unsigned voff, unsigned voff2, unsigned soff, unsigned ldst) {
  asm volatile("s_mov_b32 m0, %0\n\ts_nop 0\n\tbuffer_load_dwordx4 %1, %3, %4 offen lds\n\tbuffer_load_dwordx4 %2, %3, %4 offen offset:1024 lds"
               ::"s"(ldst), "v"(voff), "v"(voff2), "s"(rs), "s"(soff) : "memory");
}
template <int STR> DI void dma16x4(rsrc_t rs, unsigned voff, unsigned soff, unsigned ldst) {
  unsigned t;
  asm volatile("s_mov_b32 m0, %1\n\ts_add_u32 %0, %4, %5\n\tbuffer_load_dwordx4 %2, %3, %4 offen lds\n\t"
               "buffer_load_dwordx4 %2, %3, %0 offen offset:1024 lds\n\ts_add_u32 %0, %0, %5\n\t"
               "buffer_load_dwordx4 %2, %3, %0 offen offset:2048 lds\n\ts_add_u32 %0, %0, %5\n\t"
               "buffer_load_dwordx4 %2, %3, %0 offen offset:3072 lds"
               : "=&s"(t) : "s"(ldst), "v"(voff), "s"(rs), "s"(soff), "i"(STR - 1024) : "memory", "scc");
}
#define BAR() asm volatile("s_waitcnt lgkmcnt(0)\n\ts_barrier" ::: "memory")
#define BAR_VM() asm volatile("s_waitcnt vmcnt(0) lgkmcnt(0)\n\ts_barrier" ::: "memory")
#define DRAIN() asm volatile("s_waitcnt vmcnt(0)" ::: "memory")
#define SB() __builtin_amdgcn_sched_barrier(0)
#define OPQ(x) asm("" : "+v"(x))
constexpr float LOG2E = 1.4426950408889634f;
DI float phi(float t, float rs, float rl) { return __builtin_amdgcn_exp2f(fminf(t * rl, 0.f)) + fmaxf(__builtin_fmaf(t, rs, 1e-6f), 1e-6f); }
constexpr float GC = 0.4686686930532063f, GK = -4.91222953926424f;
DI float gelu_s(float v) {
  float e = __builtin_amdgcn_exp2f(v * __builtin_fmaf(-v, v, GK));
  float y = v * __builtin_amdgcn_rcpf(1.0f + e);
  OPQ(y);
  return y;
}
DI void poll_flags(unsigned char* ws, unsigned off, int idx, u64 tag) {
  const GAS u64* q = (const GAS u64*)(ws + off + idx * FS);
#pragma unroll 1
  for (unsigned it = 0; it < SPIN_CAP; ++it) {
    u64 v = __hip_atomic_load(q, __ATOMIC_RELAXED, __HIP_MEMORY_SCOPE_AGENT);
    if (__all(v == tag)) break;
    __builtin_amdgcn_s_sleep(1);
  }
  __builtin_amdgcn_fence(__ATOMIC_ACQUIRE, "wavefront");
}
DI void set_flag(unsigned char* ws, unsigned off, u64 tag) {
  __hip_atomic_store((GAS u64*)(ws + off), tag, __ATOMIC_RELAXED, __HIP_MEMORY_SCOPE_AGENT);
}
DI void set_flag_line(unsigned char* ws, unsigned off, u64 tag, int t) {
  if (t < (FULLF ? 16 : 1)) set_flag(ws, off + t * 8, tag);
}
DI v4u ld_sc1(rsrc_t rs, unsigned voff, unsigned soff) { return __builtin_amdgcn_raw_buffer_load_b128(rs, voff, soff, AUX_SC1); }
DI v2u ld_sc1_64(rsrc_t rs, unsigned voff, unsigned soff) { return __builtin_amdgcn_raw_buffer_load_b64(rs, voff, soff, AUX_SC1); }
DI void st_sc1(v4u v, rsrc_t rs, unsigned voff, unsigned soff) { __builtin_amdgcn_raw_buffer_store_b128(v, rs, voff, soff, AUX_SC1); }
DI void st_sc1_64(v2u v, rsrc_t rs, unsigned voff, unsigned soff) { __builtin_amdgcn_raw_buffer_store_b64(v, rs, voff, soff, AUX_SC1); }
DI int wrap(int v, int m) { return v >= m ? v - m : v; }

#define STAMP(k) do { } while (0)
}

__global__ void __launch_bounds__(512, 2)
fused_block(const u16* __restrict__ x, const u16* __restrict__ wqkv, const u16* __restrict__ wo, const u16* __restrict__ w1,
            const u16* __restrict__ b1, const u16* __restrict__ w2, const u16* __restrict__ b2, u16* __restrict__ out,
            unsigned char* ws) {
  __shared__ __attribute__((aligned(1024))) unsigned char smem[LDS_BYTES];
  LAS unsigned char* L = (LAS unsigned char*)smem;
  const unsigned lbase = (unsigned)(size_t)L;
  STAMP(0);
  const int tid = threadIdx.x, l = tid & 63, w = __builtin_amdgcn_readfirstlane(tid >> 6), g = l >> 4, c = l & 15;
  const int b = blockIdx.x, xcd = b & 7, slot = b >> 3;
  const int i1 = 4 * (xcd >> 1) + (slot >> 3), h = 8 * (xcd & 1) + (slot & 7);
  const int u = 4 * xcd + (slot >> 3), ct = slot & 7;
  const bool act24 = ct < 7;
  int m3 = 8 * i1 + w;
  asm volatile("" : "+s"(m3));
  const int sa = slot >> 3;
#define ROT 64
#define R1M 0
  const int r1 = R1M == 0 ? slot % 7 : R1M == 1 ? 0 : R1M == 2 ? 2 * sa : R1M == 3 ? (ct == 7 ? 0 : ct) : R1M == 4 ? (sa & 1) * 3 : R1M == 5 ? (ct & 1) * 3 : (ct >> 1) * 2, r3 = (ROT & 2) ? wrap(4 * sa + 2 * ct, 14) : 0, r4 = (ROT & 4) ? (2 * sa + ct) % 7 : 0;
  const int rka = (ROT & 8) ? sa : 0, roc = (ROT & 16) ? ct : 0, rmc = (ROT & 32) ? ct : 0, rwo = (ROT & 64) ? sa : 0;

  const rsrc_t rX = __builtin_amdgcn_make_buffer_rsrc((void*)x, 0, S_ * D_ * 2, 0x00020000);
  const rsrc_t rQ = __builtin_amdgcn_make_buffer_rsrc((void*)wqkv, 0, D_ * 3 * D_ * 2, 0x00020000);
  const rsrc_t rO = __builtin_amdgcn_make_buffer_rsrc((void*)wo, 0, D_ * D_ * 2, 0x00020000);
  const rsrc_t rA = __builtin_amdgcn_make_buffer_rsrc((void*)w1, 0, D_ * F_ * 2, 0x00020000);
  const rsrc_t rB = __builtin_amdgcn_make_buffer_rsrc((void*)w2, 0, F_ * D_ * 2, 0x00020000);
  const rsrc_t rW = __builtin_amdgcn_make_buffer_rsrc((void*)ws, 0, WS_END, 0x00020000);

  const unsigned vW = (l >> 2) * 2688 + (((l >> 1) & 1) ^ (l >> 5)) * 24 + (l & 1) * 16;
  const unsigned vXa = (16 * m3 + (l >> 3)) * 896 + ((l & 7) ^ g) * 16;
  const unsigned vXb = (vXa ^ 64) + 8 * 896;
  const unsigned sW = (32 * (w >> 2) + 16 * (w & 1)) * 2688 + (1 + ((w >> 1) & 1)) * 896 + 56 * h;
  auto issue_w = [&](int i) { dma16(rQ, vW, sW + wrap(i + r1, 7) * 64 * 2688, lbase + L_KV + w * 1024 + i * 8192); };
  auto issue_xa = [&](int i) { dma16(rX, vXa, wrap(i + r1, 7) * 128, lbase + xslot(i) + w * 2048); };
  auto issue_xb = [&](int i) { dma16(rX, vXb, wrap(i + r1, 7) * 128, lbase + xslot(i) + w * 2048 + 1024); };
  auto issue_q = [&](int m) {
    int n = w + 8 * m;
    if (m == 3) n = n >= 28 ? n - 4 : n;
    dma16(rQ, vW, (64 * wrap((n >> 2) + r1, 7) + 16 * (n & 3)) * 2688 + 56 * h, lbase + L_Q + n * 1024);
  };
  auto stage_issue = [&](int i) {
    issue_w(i);
    issue_xa(i);
    issue_xb(i);
  };
  stage_issue(0);
  const unsigned v8 = (l >> 3) * 896 + (((l >> 1) & 3) ^ g) * 32 + (l & 1) * 16 + ct * 128;
  unsigned v1off;
  {
    const int ch = 64 * (w < 7 ? w : 6) + l, row = (ch * 2341) >> 15, cc = ch - 14 * row, js = cc >> 1;
    const int j = (h & 1) ? (js == 6 ? 0 : js + 1) : js;
    v1off = row * 3584 + (112 * h + 16 * j) * 2 + (cc & 1) * 16;
  }
  const int wc = w < 7 ? w : 6;
  const int mt = w & 3, kh = w >> 2;
  unsigned l8 = (l >> 5) * 1024 + ((g & 1) * 4 + (c >> 2)) * 128 + ((g & 1) * 2 + (c >> 3)) * 32 + (c & 3) * 8;
  const int rk1 = i1 < 14 ? i1 : 13, rb2 = w < 4 || b >= 136 ? b : b + 256;
  v4u r2f;
  unsigned cpf;
  v2u ep, ep2;
  {
    u64 pc;
    unsigned sz;
    asm volatile(".Lfb_start:\n\ts_getpc_b64 %0\n\ts_mov_b32 %1, .Lfb_end-.Lfb_start" : "=s"(pc), "=s"(sz));
    const rsrc_t rC = __builtin_amdgcn_make_buffer_rsrc((void*)pc, 0, sz, 0x00020000);
    asm volatile("s_nop 4\n\tbuffer_load_dword %0, %1, %2, 0 offen" : "=v"(cpf) : "v"(w == 7 ? ((slot & 3) * 64 + l) * 128 : 0x7FFFFFF0), "s"(rC) : "memory");
    const unsigned fo = w == 6 ? (g == 0 ? WS_F1 + h * 16 * FS : g == 1 ? WS_F2 + (u >> 1) * 16 * FS : g == 2 ? WS_F3 + i1 * 16 * FS : WS_F4 + (u >> 1) * 16 * FS) + c * FS
                               : (g == 0 ? WS_FL + (h * 16 + c) * FS : WS_FR + c * 128);
    asm volatile("buffer_load_dwordx2 %0, %1, %2, 0 offen sc1" : "=v"(ep) : "v"(PRET && (w == 5 || w == 6) ? fo : 0u), "s"(rW) : "memory");
    asm volatile("buffer_load_dwordx2 %0, off, %1, 0 sc1" : "=v"(ep2) : "s"(rW) : "memory");
  }
  STAMP(1);
  const unsigned lq0 = (l >> 5) * 1024 + (g & 1) * 544 + (c >> 2) * 64 + (c & 3) * 8, lq1 = lq0 ^ 32;
  unsigned lx0 = xslot(0) + w * 2048 + c * 128 + (g ^ (c >> 1)) * 16, lx1 = lx0 ^ 64;
  OPQ(lx0);
  OPQ(lx1);
  const unsigned xw = L_X + (w >> 1) * 1024 + l * 16 + (w & 1) * 8;
  v4u xf[14];
  f32x4 acc[4], as1 = {0.f, 0.f, 0.f, 0.f}, as2 = as1, acs = as1;
  const unsigned csa = (lq0 ^ ((w & 1) * 32)) + L_KV + (w >> 2) * 4096 + ((w >> 1) & 1) * 2048;
#pragma unroll
  for (int t = 0; t < 4; ++t) acc[t] = f32x4{0.f, 0.f, 0.f, 0.f};
  const unsigned ONES = 0x3F803F80u;
  const v4u on4 = {ONES, ONES, ONES, ONES};
  u64 tag = 0;
  float rs[4], mn[4];
  if (tid == 0) *(LAS unsigned*)(L + L_CNT) = 0u;
  v4u f[2][8], fc[2];
  auto mm = [&](int i, int n) {
    const int ss = n / 6, r = n % 6;
    if (n == 12) acs = mfma(on4, fc[i & 1], acs);
    else if (r == 0) as1 = mfma(xf[2 * i + ss], on4, as1);
    else if (r == 1) as2 = mfma(xf[2 * i + ss], xf[2 * i + ss], as2);
    else acc[r - 2] = mfma(xf[2 * i + ss], f[i & 1][4 * ss + r - 2], acc[r - 2]);
  };
  auto rd = [&](int i, int n) {
    if (n == 0) xf[2 * i] = *(LAS v4u*)(L + (i & 3) * 16 * K + lx0);
    else if (n == 5) xf[2 * i + 1] = *(LAS v4u*)(L + (i & 3) * 16 * K + lx1);
    else if (n == 10) fc[i & 1] = trfrag(L, csa + i * 8192, 256);
    else {
      const int q = n < 5 ? n - 1 : n - 2;
      f[i & 1][q] = trfrag(L, ((q & 1) ? lq1 : lq0) + L_KV + (2 * i + (q >> 2)) * 4096 + ((q >> 1) & 1) * 2048, 256);
    }
  };
  float cs0[4];
  v4u g2a, fc6;
#pragma unroll
  for (int i = 0; i < 7; ++i) {
    constexpr int NW[7] = {3, 7, 6, 6, 7, 6, 4};
    asm volatile("s_waitcnt vmcnt(%3) lgkmcnt(0)\n\ts_barrier" : "+v"(ep), "+v"(ep2), "+v"(cpf) : "i"(NW[i]) : "memory");
    if (i == 0) STAMP(13);
    if (i == 3) STAMP(16);
    if (i == 6) STAMP(9);
#pragma unroll
    for (int n = 0; n < 13; ++n) {
      if (n < (i < 6 ? 11 : 10)) rd(i, n);
      if (i > 0 && i < 6) mm(i - 1, n);
      if (i == 0) {
        if (n == 0) stage_issue(1);
        if (n == 1) issue_w(2);
        if (n == 2) issue_w(3);
        if (n == 3) issue_xa(2);
        if (n == 4) issue_xb(2);
        if (n == 5) issue_w(4);
        if (n == 6) issue_xa(3);
        if (n == 7) issue_xb(3);
      }
      if (i == 1 || i == 2) {
        if (n == 1) issue_w(i + 4);
        if (n == 5) issue_xa(i + 3);
        if (n == 9) issue_xb(i + 3);
      }
      if (i == 3) {
        if (n == 1) issue_xa(6);
        if (n == 4) issue_xb(6);
        if (n == 7) dma16(rA, v1off, rk1 * 32 * 3584, lbase + L_RP + 8 * K + wc * 1024);
        if (n == 10) dma16(rB, v8 - ct * 128, (32 * (rb2 % 56) + 8 * (w & 3)) * 896 + (rb2 / 56) * 128, lbase + L_RP + w * 1024);
      }
      if (i == 4 || i == 5) {
        if (n == 1) issue_q(2 * i - 8);
        if (n == 7) issue_q(2 * i - 7);
      }
      if (i == 5 && n == 11) fc6 = trfrag(L, csa + 6 * 8192, 256);
      if (i == 6 && n >= 4 && n < 12) mm(6, n < 8 ? (n & 1) + 6 * ((n - 4) >> 1) : n - 6);
      SB();
    }
    if (i == 5) {
      mm(5, 12);
      acs = mfma(on4, fc6, acs);
#pragma unroll
      for (int n = 0; n < 6; ++n) mm(5, n);
      if (g == 0) *(LAS float*)(L + L_CS1 + w * 64 + c * 4) = acs[0];
#pragma unroll
      for (int n = 6; n < 12; ++n) mm(5, n);
    }
    if (i == 2) {
      const u64 e = ((u64)(unsigned)__builtin_amdgcn_readfirstlane(ep2.y) << 32) | (unsigned)__builtin_amdgcn_readfirstlane(ep2.x);
      tag = e * 6364136223846793005ull + 1442695040888963407ull;
    }
    SB();
  }
  float dg1, dg2;
  {
#pragma unroll
    for (int t = 0; t < 4; ++t) cs0[t] = *(LAS float*)(L + L_CS1 + t * 64 + c * 4) + *(LAS float*)(L + L_CS1 + (t + 4) * 64 + c * 4);
    {
      const unsigned ra = ((l >> 5) * 8 + (g & 1) * 4 + (c >> 2)) * 224 + (c & 3) * 8 + L_RP + 8 * K;
      g2a = trfrag(L, ra + wc * 32, 3584);
      r2f = trfrag(L, (l8 ^ ((w & 3) * 32)) + L_RP + (w >> 2) * 4096, 2048);
    }
    const bool b0 = c & 1, b1 = c & 2, dg = g == (c >> 2);
    dg2 = b1 ? (b0 ? as2[3] : as2[2]) : (b0 ? as2[1] : as2[0]);
    dg2 = dg ? dg2 : 0.f;
    const f32x4 a2 = rowsum(dg2);
    SB();
#pragma unroll
    for (int n = 8; n < 12; ++n) mm(6, n);
#pragma unroll
    for (int r = 0; r < 4; ++r) {
      mn[r] = as1[r] * (-1.0f / 448.0f);
      rs[r] = __builtin_amdgcn_rsqf(fmaxf(__builtin_fmaf(a2[r], 1.0f / 448.0f, -mn[r] * mn[r]), 0.f) + 1e-5f);
    }
    dg1 = b1 ? (b0 ? as1[3] : as1[2]) : (b0 ? as1[1] : as1[0]);
    dg1 = dg ? dg1 : 0.f;
  }
  STAMP(2);
#pragma unroll
  for (int t = 0; t < 4; ++t) {
    float v[4];
#pragma unroll
    for (int r = 0; r < 4; ++r) {
      const float d = __builtin_fmaf(mn[r], cs0[t], acc[t][r]);
      v[r] = t < 2 ? phi(d, rs[r], rs[r] * LOG2E) : d * rs[r];
      if (t == 3) v[r] = c < 4 ? 1.0f : v[r];
      OPQ(v[r]);
    }
    *(LAS v2u*)(L + xw + t * 4096) = v2u{pack2(v[0], v[1]), pack2(v[2], v[3])};
  }
  BAR_VM();
  STAMP(3);
  if (w < 4) {
    f32x4 kv = {0.f, 0.f, 0.f, 0.f};
    v4u a[4], bb[4];
#pragma unroll
    for (int ks = 0; ks < 4; ++ks) {
      a[ks] = *(LAS v4u*)(L + L_X + (((w >> 1) * 4 + ks) * 64 + l) * 16);
      bb[ks] = *(LAS v4u*)(L + L_X + (((2 + (w & 1)) * 4 + ks) * 64 + l) * 16);
    }
#pragma unroll
    for (int ks = 0; ks < 4; ++ks) kv = mfma(a[ks], bb[ks], kv);
    if (w >= 2 && g == 0) kv = f32x4{0.f, 0.f, 0.f, 0.f};
    st_sc1(__builtin_bit_cast(v4u, kv), rW, l * 16, WS_KVP + ((h * 4 + w) * 16 + i1) * 1024);
  }
  v4u pqf;
  v2u xr0, xr1, bb1;
  {
    constexpr int QB = QBV;
    f32x4 aq[2] = {f32x4{0.f, 0.f, 0.f, 0.f}, f32x4{0.f, 0.f, 0.f, 0.f}};
    f32x4 csq = {0.f, 0.f, 0.f, 0.f};
    unsigned lqa = lq0 + L_Q, lqb = lq1 + L_Q;
    OPQ(lqa);
    OPQ(lqb);
    const unsigned qa = (lqa ^ ((w & 1) * 32)) + (w >> 1) * 2048;
    v4u f[28], fc[4];
#pragma unroll
    for (int q = 0; q < 6; ++q) f[q] = trfrag(L, ((q & 1) ? lqb : lqa) + (q >> 1) * 2048, 256);
    SB();
    const unsigned on3 = w < 4 ? ONES : 0u;
    const v4u on43 = {on3, on3, on3, on3};
    unsigned kvo[4];
#pragma unroll
    for (int n = 0; n < 4; ++n) kvo[n] = WS_KVP + ((h * 4 + (w & 3)) * 16 + 8 * (w >> 2) + 2 * ((n + rka) & 3)) * 1024;
    u64 early = 0;
#pragma unroll
    for (int q = 0; q < 28; ++q) {
      if (q + 6 < 28) f[q + 6] = trfrag(L, ((q & 1) ? lqb : lqa) + ((q + 6) >> 1) * 2048, 256);
      if (q < 4) fc[q] = trfrag(L, qa + (q < 3 ? q * 8192 : (w < 4 ? 3 * 8192 : 0)), 256);
      aq[q & 1] = mfma(f[q], xf[q >> 1], aq[q & 1]);
      if (q >= 6 && q < 10) csq = mfma(q < 9 ? on4 : on43, fc[q - 6], csq);
      if (q == QB && w < 4) {
        DRAIN();
        unsigned old = 0;
        if (l == 0) old = __hip_atomic_fetch_add((LAS unsigned*)(L + L_CNT), 1u, __ATOMIC_RELAXED, __HIP_MEMORY_SCOPE_WORKGROUP);
        if (__builtin_amdgcn_readfirstlane(old) == 3u) set_flag_line(ws, WS_F1 + (h * 16 + i1) * FS, tag, l);
        STAMP(4);
      }
      if (q % 4 == 0 && q < 20 && w >= 4 && w < 7) {
        const int gi = wrap((w - 4) * 5 + q / 4 + 4 * rwo, 14);
        if (w < 6 || q < 16) dma16x4<8 * 896>(rO, v8, gi * 32 * 896, lbase + L_WO + gi * 4096);
      }
      if (q == 18 && g == 0) *(LAS float*)(L + L_CSQ + w * 64 + c * 4) = csq[0];
      if (q == 24 && w == 7) early = __hip_atomic_load((const GAS u64*)(ws + WS_F1 + (h * 16 + c) * FS), __ATOMIC_RELAXED, __HIP_MEMORY_SCOPE_AGENT);
      SB();
    }
    auto rstore = [&]() {
      st_sc1(g2a, rW, l * 16, WS_R1 + ((h * 14 + rk1) * 7 + wc) * 1024);
      st_sc1(r2f, rW, l * 16, WS_R2 + (rb2 * 4 + (w & 3)) * 1024);
    };
    if (w < 7) rstore();
    const float mq = colsum(dg1) * (-1.0f / 448.0f);
    const float rq = __builtin_amdgcn_rsqf(fmaxf(__builtin_fmaf(colsum(dg2), 1.0f / 448.0f, -mq * mq), 0.f) + 1e-5f);
    STAMP(5);
    if (w == 7 && !__all(early == tag)) poll_flags(ws, WS_F1 + h * 16 * FS, c, tag);
    BAR();
    STAMP(6);
    v4u p[8];
#pragma unroll
    for (int n = 0; n < 8; ++n) {
      p[n] = ld_sc1(rW, l * 16 + (n & 1) * 1024, kvo[n >> 1]);
      SB();
    }
    if (w == 7) rstore();
    const unsigned vxr = (64 * u + 16 * mt + c) * 896 + (64 * ct + 32 * kh + 4 * g) * 2;
    xr0 = __builtin_amdgcn_raw_buffer_load_b64(rX, vxr, 0, 0);
    xr1 = __builtin_amdgcn_raw_buffer_load_b64(rX, vxr + 32, 0, 0);
    bb1 = *(const GAS v2u*)(b1 + 112 * h + 16 * ((h & 1) ? (wc == 6 ? 0 : wc + 1) : wc) + 4 * g);
    SB();
#pragma unroll
    for (int t = 0; t < 2; ++t) {
      f32x4 cs = *(LAS f32x4*)(L + L_CSQ + t * 64 + g * 16);
#pragma unroll
      for (int m = 1; m < 4; ++m) cs += *(LAS f32x4*)(L + L_CSQ + (t + 2 * m) * 64 + g * 16);
      float v[4];
#pragma unroll
      for (int r = 0; r < 4; ++r) v[r] = phi(__builtin_fmaf(mq, cs[r], aq[t][r]), rq, rq * LOG2E);
      pqf[2 * t] = pack2(v[0], v[1]);
      pqf[2 * t + 1] = pack2(v[2], v[3]);
    }
    asm volatile("" : "+v"(pqf));
    SB();
    f32x4 sum = __builtin_bit_cast(f32x4, p[0]);
#pragma unroll
    for (int n = 1; n < 8; ++n) sum += __builtin_bit_cast(f32x4, p[n]);
    *(LAS f32x4*)(L + L_KVS + w * 1024 + l * 16) = sum;
  }
  BAR_VM();
  set_flag_line(ws, WS_FL + (h * 16 + i1) * FS, tag, tid);
  if (tid == 64) set_flag(ws, WS_FR + (h * 16 + i1) * 8, tag);
  STAMP(7);
  {
    f32x4 T[4], Z[2];
#pragma unroll
    for (int dt = 0; dt < 2; ++dt)
      Z[dt] = *(LAS f32x4*)(L + L_KVS + (dt * 2 + 1) * 1024 + g * 256) + *(LAS f32x4*)(L + L_KVS + (dt * 2 + 5) * 1024 + g * 256);
#pragma unroll
    for (int t = 0; t < 4; ++t) T[t] = *(LAS f32x4*)(L + L_KVS + t * 1024 + l * 16) + *(LAS f32x4*)(L + L_KVS + (t + 4) * 1024 + l * 16);
    v4u f0 = {pack2(T[0].x, T[0].y), pack2(T[0].z, T[0].w), pack2(T[2].x, T[2].y), pack2(T[2].z, T[2].w)};
    v4u f1 = {pack2(T[1].x, T[1].y), pack2(T[1].z, T[1].w), pack2(T[3].x, T[3].y), pack2(T[3].z, T[3].w)};
    v4u fz = {pack2(Z[0].x, Z[0].y), pack2(Z[0].z, Z[0].w), pack2(Z[1].x, Z[1].y), pack2(Z[1].z, Z[1].w)};
    const f32x4 z4 = {0.f, 0.f, 0.f, 0.f};
    f32x4 den = mfma(fz, pqf, z4), n0 = mfma(f0, pqf, z4), n1 = mfma(f1, pqf, z4);
    const float rd = __builtin_amdgcn_rcpf(den.x + 1e-6f);
    const float rd1 = g == 0 ? 0.f : rd;
    v4u of = {pack2(n0.x * rd, n0.y * rd), pack2(n0.z * rd, n0.w * rd), pack2(n1.x * rd1, n1.y * rd1), pack2(n1.z * rd1, n1.w * rd1)};
    st_sc1(of, rW, l * 16, WS_O + (m3 * 16 + h) * 1024);
  }
  BAR_VM();
  set_flag_line(ws, WS_F2 + (i1 * 16 + h) * FS, tag, tid);
  u64 earlyr = 0;
  if (w == 7) earlyr = __hip_atomic_load((const GAS u64*)(ws + (FRL ? WS_FL + (h * 16 + c) * FS : WS_FR + h * 128 + c * 8)), __ATOMIC_RELAXED, __HIP_MEMORY_SCOPE_AGENT);
  STAMP(8);
  asm volatile("" : "+v"(xr0), "+v"(xr1), "+v"(bb1));
  unsigned w1A = WS_R1 + ((h * 14 + r3) * 7 + w) * 1024, w1B = w1A - 14 * 7168, w1D = lbase + L_W1 + w * 1024;
  asm volatile("" : "+s"(w1A), "+s"(w1B), "+s"(w1D));
  auto w1dma = [&](int s) {
    dma16s(rW, l * 16, (r3 < 14 - s ? w1A : w1B) + s * 7168, w1D + s * 7168);
  };
  constexpr int W1A = 8;
  auto w1early = [&]() {
    if (w == 7 && !__all(earlyr == tag)) poll_flags(ws, FRL ? WS_FL + (h * 16 + c) * FS : WS_FR + h * 128 + c * 8, 0, tag);
    asm volatile("s_barrier" ::: "memory");
    if (w < 7) {
#pragma unroll
      for (int s = 0; s < W1A; ++s) w1dma(s);
    }
  };
  v4u hres;
  if (act24) {
    unsigned wb[4];
#pragma unroll
    for (int j = 0; j < 4; ++j) wb[j] = (l8 ^ (kh * 64) ^ (j * 32)) + L_WO + kh * (224 * 128);
    auto wfrag = [&](int q) {
      const int n = q >> 2, jl = q & 3;
      const unsigned po = (((n >> 1) + roc) & 3) * (56 * 128) + (n & 1) * (28 * 128);
      return trfrag2(L, wb[jl ^ ((n & 1) * 2)] + po, wb[jl ^ (((n & 1) ^ 1) * 2)] + po + 12 * 128);
    };
    unsigned oo[4];
#pragma unroll
    for (int n = 0; n < 4; ++n) {
      oo[n] = WS_O + ((4 * u + mt) * 16 + 8 * kh + 2 * ((n + roc) & 3)) * 1024;
      asm volatile("" : "+s"(oo[n]));
    }
    v4u wf[32];
#pragma unroll
    for (int q = 0; q < 32; ++q) wf[q] = wfrag(q);
    SB();
    w1early();
    const int m2 = 4 * u + mt;
    f32x4 a2[4] = {f32x4{bflo(xr0.x), bfhi(xr0.x), bflo(xr0.y), bfhi(xr0.y)}, f32x4{bflo(xr1.x), bfhi(xr1.x), bflo(xr1.y), bfhi(xr1.y)},
                   f32x4{0.f, 0.f, 0.f, 0.f}, f32x4{0.f, 0.f, 0.f, 0.f}};
    asm volatile("" : "+v"(a2[0]), "+v"(a2[1]));
    if (w == 7) {
      const GAS u64* q = (const GAS u64*)(ws + WS_F2 + ((u >> 1) * 16 + c) * FS);
      const unsigned tl = (unsigned)tag, th = (unsigned)(tag >> 32);
#pragma unroll 1
      for (unsigned it = 0; it < SPIN_CAP; ++it) {
        const u64 v = __hip_atomic_load(q, __ATOMIC_RELAXED, __HIP_MEMORY_SCOPE_AGENT);
        const v4u fa = ld_sc1(rW, l * 32, WS_FR), fb = ld_sc1(rW, l * 32 + 16, WS_FR);
        if (__all(v == tag && fa.x == tl && fa.y == th && fa.z == tl && fa.w == th && fb.x == tl && fb.y == th && fb.z == tl && fb.w == th)) break;
        asm volatile("s_sleep 1" ::: "memory");
      }
    }
    BAR();
    STAMP(10);
    v4u of[8];
    auto oload = [&](int n) { of[n] = ld_sc1(rW, l * 16 + (n & 1) * 1024, oo[n >> 1]); };
#pragma unroll
    for (int n = 0; n < 8; ++n) {
      oload(n);
      SB();
    }
#pragma unroll
    for (int q = 0; q < 32; ++q) {
      if (q == 12) asm volatile("" : "+v"(of[3]), "+v"(of[4]), "+v"(of[5]), "+v"(of[6]), "+v"(of[7]));
      if (q >= 12 && q % 3 == 0 && W1A + (q - 12) / 3 < 14 && w < 7) w1dma(W1A + (q - 12) / 3);
      a2[q & 3] = mfma(wf[q], of[q >> 2], a2[q & 3]);
      SB();
    }
    *(LAS f32x4*)(L + L_EX + w * 2048 + l * 16) = a2[2];
    *(LAS f32x4*)(L + L_EX + w * 2048 + 1024 + l * 16) = a2[3];
    BAR();
    STAMP(11);
    a2[0] += *(LAS f32x4*)(L + L_EX + (w ^ 4) * 2048 + l * 16);
    a2[1] += *(LAS f32x4*)(L + L_EX + (w ^ 4) * 2048 + 1024 + l * 16);
    hres = v4u{pack2(a2[0].x, a2[0].y), pack2(a2[0].z, a2[0].w), pack2(a2[1].x, a2[1].y), pack2(a2[1].z, a2[1].w)};
    st_sc1(hres, rW, l * 16, WS_H + (m2 * 16 + 2 * ct + kh) * 1024);
    const f32x4 z4 = {0.f, 0.f, 0.f, 0.f};
    const f32x4 gm = mfma(hres, hres, z4);
    const float t1 = mfma(on4, hres, z4)[0];
    const float t2 = (c & 2) ? ((c & 1) ? gm[3] : gm[2]) : ((c & 1) ? gm[1] : gm[0]);
    if (g == (c >> 2)) st_sc1_64(v2u{asu(t1), asu(t2)}, rW, c * 8, WS_ST + (m2 * 16 + 2 * ct + kh) * 128);
    BAR_VM();
    set_flag_line(ws, WS_F3 + (u * 8 + ct) * FS, tag, tid);
    if (b == 0 && tid == 64) set_flag(ws, WS_EPOCH, tag);
  } else {
    w1early();
    if (w < 7) {
#pragma unroll
      for (int s = W1A; s < 14; ++s) w1dma(s);
    }
    BAR_VM();
  }
  STAMP(12);
  v4u wf2[7][4];
  unsigned w2o[7];
#pragma unroll
  for (int kk = 0; kk < 7; ++kk) {
    w2o[kk] = WS_R2 + (ct * 56 + 7 * w + wrap(kk + r4, 7)) * 4096;
    asm volatile("" : "+s"(w2o[kk]));
  }
  auto w2load = [&](int i) { wf2[i >> 2][i & 3] = ld_sc1(rW, l * 16 + (i & 3) * 1024, w2o[i >> 2]); };
  unsigned lP3 = L_W1 + l * 16, lP3b = lP3 + 7 * 7168;
  OPQ(lP3b);
  {
    v4u f[14];
#pragma unroll
    for (int s = 0; s < 14; ++s) f[s] = *(LAS v4u*)(L + (s < 7 ? lP3 + s * 7168 : lP3b + (s - 7) * 7168) + wc * 1024);
    SB();
    f32x4 cs = {0.f, 0.f, 0.f, 0.f};
#pragma unroll
    for (int s = 0; s < 14; ++s) {
      cs = mfma(f[s], on4, cs);
      if (s < W2_A) w2load(s);
      SB();
    }
    if (c == 0) {
      *(LAS f32x4*)(L + L_C1 + wc * 64 + g * 16) = cs * GC;
      *(LAS f32x4*)(L + L_C1 + 448 + wc * 64 + g * 16) = f32x4{bflo(bb1.x), bfhi(bb1.x), bflo(bb1.y), bfhi(bb1.y)} * GC;
    }
  }
  {
    auto fs = [](int q) { return q < 56 ? q >> 2 : (q - 56) % 14; };
    auto fj = [](int q) { return q < 56 ? q & 3 : 4 + (q - 56) / 14; };
    auto frag = [&](int q) {
      const int s = fs(q), j = fj(q);
      return *(LAS v4u*)(L + (s < 7 ? lP3 + s * 7168 : lP3b + (s - 7) * 7168) + j * 1024);
    };
    constexpr int PF3 = 6;
    v4u f[98];
#pragma unroll
    for (int q = 0; q < PF3; ++q) f[q] = frag(q);
    unsigned ho[14];
#pragma unroll
    for (int s = 0; s < 14; ++s) {
      ho[s] = WS_H + (m3 * 16 + wrap(s + r3, 14)) * 1024;
      asm volatile("" : "+s"(ho[s]));
    }
    if (w == 7) poll_flags(ws, WS_F3 + i1 * 16 * FS, (c & 7) == 7 ? 0 : c, tag);
    BAR();
    STAMP(14);
    v4u hf[14];
    auto hld = [&](int s) { hf[s] = ld_sc1(rW, l * 16, ho[s]); };
    constexpr int HA = HAV;
#pragma unroll
    for (int s = 0; s < HA; ++s) {
      hld(s);
      SB();
    }
    v2u st[4];
    f32x4 a3[7];
#pragma unroll
    for (int j = 0; j < 7; ++j) a3[j] = f32x4{0.f, 0.f, 0.f, 0.f};
#pragma unroll
    for (int q = 0; q < 56; ++q) {
      f[q + PF3] = frag(q + PF3);
      a3[fj(q)] = mfma(f[q], hf[fs(q)], a3[fj(q)]);
      if (q % 4 == 0 && q / 4 + HA < 14) hld(q / 4 + HA);
      if (q >= 8 && q < 12) st[q - 8] = ld_sc1_64(rW, c * 8 + g * 128 + (q - 8) * 512, WS_ST + m3 * 2048);
      if (q >= 32 && q % 4 == 0) w2load(W2_A + (q - 32) / 4);
      SB();
    }
    STAMP(20);
    float t1 = asf(st[0].x) + asf(st[1].x) + asf(st[2].x) + (g < 2 ? asf(st[3].x) : 0.f);
    float t2 = asf(st[0].y) + asf(st[1].y) + asf(st[2].y) + (g < 2 ? asf(st[3].y) : 0.f);
    const float mean2 = colsum(t1) * (1.0f / 448.0f);
    const float rs2 = __builtin_amdgcn_rsqf(fmaxf(__builtin_fmaf(colsum(t2), 1.0f / 448.0f, -mean2 * mean2), 0.f) + 1e-5f);
    const float nm2 = -mean2 * rs2, rs2c = rs2 * GC;
    const unsigned ksp = (7 * h + 1) >> 1;
    const unsigned ob = WS_H1 + (m3 * 56 + ksp) * 1024;
    unsigned pk[14];
    auto epi1 = [&](int j, int r, const f32x4& cs, const f32x4& bb) {
      return gelu_s(__builtin_fmaf(a3[j][r], rs2c, __builtin_fmaf(nm2, cs[r], bb[r])));
    };
#pragma unroll
    for (int jj = 0; jj < 7; ++jj) {
      f32x4 cs = *(LAS f32x4*)(L + L_C1 + jj * 64 + g * 16);
      f32x4 bb = *(LAS f32x4*)(L + L_C1 + 448 + jj * 64 + g * 16);
      SB();
      float y[4];
#pragma unroll
      for (int s = 0; s < 7; ++s) {
        const int q = 56 + jj * 7 + s;
        if (jj < 6) {
          if (q + PF3 < 98) f[q + PF3] = frag(q + PF3);
          a3[fj(q)] = mfma(f[q], hf[fs(q)], a3[fj(q)]);
          if ((s == 1 || s == 4) && 6 + jj * 2 + s / 4 < W2_B) w2load(W2_A + 6 + jj * 2 + s / 4);
        }
        if (s == 2 || s == 4) {
          y[s - 2] = epi1(jj, s - 2, cs, bb);
          y[s - 1] = epi1(jj, s - 1, cs, bb);
        }
        if (s == 3 || s == 5) pk[2 * jj + (s - 3) / 2] = pack2(y[s - 3], y[s - 2]);
        if (s != 2 && s != 4) SB();
      }
      if (jj & 1) st_sc1(v4u{pk[2 * jj - 2], pk[2 * jj - 1], pk[2 * jj], pk[2 * jj + 1]}, rW, l * 16 + (jj >> 1) * 1024, ob);
    }
    if (STRAD) st_sc1_64(v2u{pk[12], pk[13]}, rW, l * 8, (h & 1) ? ob - 512 : ob + 3072);
    else st_sc1_64(v2u{pk[12], pk[13]}, rW, l * 16, (h & 1) ? ob - 1016 : ob + 3072);
    STAMP(21);
    BAR_VM();
    set_flag_line(ws, WS_F4 + (i1 * 16 + h) * FS, tag, tid);
  }
  STAMP(15);
  if (act24) {
#pragma unroll
    for (int i = W2_A + W2_B; i < 28; ++i) w2load(i);
    v4u bb2 = *(const GAS v4u*)(b2 + 64 * ct + (tid & 7) * 8);
    const unsigned bb2s = *(const GAS unsigned*)(b2 + 64 * ct + (tid & 31) * 2);
    *(LAS v2u*)(L + L_HT + (16 * mt + c) * 144 + (32 * kh + 4 * g) * 2) = v2u{hres.x, hres.y};
    *(LAS v2u*)(L + L_HT + (16 * mt + c) * 144 + (32 * kh + 16 + 4 * g) * 2) = v2u{hres.z, hres.w};
    unsigned ko[7], mo[4];
#pragma unroll
    for (int kk = 0; kk < 7; ++kk) {
      ko[kk] = l * 16 + wrap(kk + r4, 7) * 1024;
      asm volatile("" : "+v"(ko[kk]));
    }
#pragma unroll
    for (int m = 0; m < 4; ++m) {
      mo[m] = WS_H1 + ((4 * u + ((m + rmc) & 3)) * 56 + 7 * w) * 1024;
      asm volatile("" : "+s"(mo[m]));
    }
    if (w == 7) poll_flags(ws, WS_F4 + (u >> 1) * 16 * FS, c, tag);
    BAR();
    STAMP(17);
    v4u hf[28];
    auto hload = [&](int i) {
      const int m = i / 7, kk = i % 7;
      if (STRAD && kk == 3) {
        const v2u lo = ld_sc1_64(rW, l * 8 + 3072, mo[m]), hi = ld_sc1_64(rW, l * 8 + 3584, mo[m]);
        hf[i] = v4u{lo.x, lo.y, hi.x, hi.y};
      } else {
        hf[i] = ld_sc1(rW, ko[kk], mo[m]);
      }
    };
    constexpr int HB = HBV;
#pragma unroll
    for (int i = 0; i < HB; ++i) {
      hload(i);
      SB();
    }
    f32x4 a4[4][4];
    auto ptw = [&](int m, int j) { *(LAS f32x4*)(L + L_PT + w * 17408 + ((m + rmc) & 3) * (16 * 272) + c * 272 + g * 16 + j * 64) = a4[m][j]; };
    constexpr float IG = 1.0f / GC;
    const unsigned pa = L_PT + (tid >> 3) * 272 + (tid & 7) * 32;
    f32x4 o0 = {0.f, 0.f, 0.f, 0.f}, o1 = o0;
#pragma unroll
    for (int m = 0; m < 4; ++m) {
#pragma unroll
      for (int j = 0; j < 4; ++j) a4[m][j] = f32x4{0.f, 0.f, 0.f, 0.f};
#pragma unroll
      for (int kk = 0; kk < 7; ++kk) {
        if (7 * m + kk + HB < 28) hload(7 * m + kk + HB);
        if (m > 0 && m < 3 && kk >= 1 && kk <= 4) ptw(m - 1, kk - 1);
        if (m == 3 && kk < 2) {
          ptw(2, 2 * kk);
          ptw(2, 2 * kk + 1);
        }
        if (TAIL && m == 3 && kk == 3) BAR();
        f32x4 t[4];
        if (TAIL && m == 3 && kk >= 3 && w < 6) {
#pragma unroll
          for (int e = 0; e < 2; ++e) {
            t[2 * e] = *(LAS f32x4*)(L + pa + (2 * (kk - 3) + e) * 17408);
            t[2 * e + 1] = *(LAS f32x4*)(L + pa + (2 * (kk - 3) + e) * 17408 + 16);
          }
        }
#pragma unroll
        for (int j = 0; j < 4; ++j) a4[m][j] = mfma(wf2[kk][j], hf[7 * m + kk], a4[m][j]);
        SB();
        if (TAIL && m == 3 && kk >= 3 && w < 6) {
          o0 += t[0];
          o1 += t[1];
          o0 += t[2];
          o1 += t[3];
        }
        SB();
      }
    }
#pragma unroll
    for (int j = 0; j < 4; ++j) ptw(3, j);
    auto fin = [&]() {
      v4u hh = *(LAS v4u*)(L + L_HT + (tid >> 3) * 144 + (tid & 7) * 16);
      v4u ov = {pack2(o0.x * IG + (bflo(bb2.x) + bflo(hh.x)), o0.y * IG + (bfhi(bb2.x) + bfhi(hh.x))),
                pack2(o0.z * IG + (bflo(bb2.y) + bflo(hh.y)), o0.w * IG + (bfhi(bb2.y) + bfhi(hh.y))),
                pack2(o1.x * IG + (bflo(bb2.z) + bflo(hh.z)), o1.y * IG + (bfhi(bb2.z) + bfhi(hh.z))),
                pack2(o1.z * IG + (bflo(bb2.w) + bflo(hh.w)), o1.w * IG + (bfhi(bb2.w) + bfhi(hh.w)))};
      *(GAS v4u*)(out + (64 * u + (tid >> 3)) * 448 + 64 * ct + (tid & 7) * 8) = ov;
    };
    if (TAIL) {
      const unsigned hb = *(LAS unsigned*)(L + L_HT + (48 + (tid >> 5)) * 144 + (tid & 31) * 4);
      if (w < 6) fin();
      BAR();
      STAMP(18);
      const unsigned pb = L_PT + (48 + (tid >> 5)) * 272 + (tid & 31) * 8;
      f32x2 r[8];
#pragma unroll
      for (int ww = 0; ww < 8; ++ww) r[ww] = *(LAS f32x2*)(L + pb + ww * 17408);
      const f32x2 q = ((r[0] + r[1]) + (r[2] + r[3])) + ((r[4] + r[5]) + (r[6] + r[7]));
      *(GAS unsigned*)(out + (64 * u + 48 + (tid >> 5)) * 448 + 64 * ct + (tid & 31) * 2) =
          pack2(q.x * IG + (bflo(bb2s) + bflo(hb)), q.y * IG + (bfhi(bb2s) + bfhi(hb)));
    } else {
      BAR();
      STAMP(18);
#pragma unroll
      for (int ww = 0; ww < 8; ++ww) {
        o0 += *(LAS f32x4*)(L + pa + ww * 17408);
        o1 += *(LAS f32x4*)(L + pa + ww * 17408 + 16);
      }
      fin();
    }
  }
  STAMP(19);
  asm volatile(".Lfb_end:" ::: "memory");
}

extern "C" void kernel_launch(void* const* d_in, const int* in_sizes, int n_in, void* d_out, int out_size, void* d_ws,
                              size_t ws_size, hipStream_t stream) {
  (void)in_sizes; (void)n_in; (void)out_size; (void)ws_size;
  fused_block<<<256, 512, 0, stream>>>((const u16*)d_in[0], (const u16*)d_in[1], (const u16*)d_in[2], (const u16*)d_in[4],
                                       (const u16*)d_in[5], (const u16*)d_in[6], (const u16*)d_in[7], (u16*)d_out,
                                       (unsigned char*)d_ws);
}
```

```cpp
#include <hip/hip_runtime.h>
#include <hip/hip_bf16.h>

#define LAS __attribute__((address_space(3)))
#define GAS __attribute__((address_space(1)))
#define DI __device__ __forceinline__
typedef unsigned v4u __attribute__((ext_vector_type(4)));
typedef unsigned v2u __attribute__((ext_vector_type(2)));
typedef float f32x4 __attribute__((ext_vector_type(4)));
typedef float f32x2 __attribute__((ext_vector_type(2)));
typedef short s16x4 __attribute__((ext_vector_type(4)));
typedef __bf16 bf16x8 __attribute__((ext_vector_type(8)));
typedef __bf16 bf16x2 __attribute__((ext_vector_type(2)));
typedef unsigned long long u64;
typedef unsigned short u16;
typedef __amdgpu_buffer_rsrc_t rsrc_t;

namespace {
constexpr int S_ = 2048, D_ = 448, F_ = 1792;
constexpr unsigned FS = 128;
constexpr unsigned WS_EPOCH = 0;
constexpr unsigned WS_F1 = 4096;
constexpr unsigned WS_F2 = WS_F1 + 256 * FS;
constexpr unsigned WS_F3 = WS_F2 + 256 * FS;
constexpr unsigned WS_F4 = WS_F3 + 256 * FS;
constexpr unsigned WS_FL = WS_F4 + 256 * FS;
constexpr unsigned WS_FR = WS_FL + 256 * FS;
constexpr unsigned WS_KVP = 262144;
constexpr unsigned WS_O = WS_KVP + (1u << 20);
constexpr unsigned WS_H = WS_O + (2u << 20);
constexpr unsigned WS_ST = WS_H + (2u << 20);
constexpr unsigned WS_H1 = WS_ST + (256u << 10);
constexpr unsigned WS_R1 = WS_H1 + 128u * 56u * 1024u;
constexpr unsigned WS_R2 = WS_R1 + 16u * 14u * 7168u;
constexpr unsigned WS_END = WS_R2 + 7u * 56u * 4096u;
constexpr unsigned K = 1024;
constexpr unsigned L_KV = 0, L_Q = 56 * K, L_X = 84 * K, L_WO = 100 * K, L_SC = 156 * K;
constexpr unsigned L_CSQ = L_SC, L_CS1 = L_SC + 512, L_C1 = L_SC + 1024, L_CNT = L_SC + 2048;
constexpr unsigned L_EX = 104 * K, L_KVS = 88 * K, L_RP = 0;
constexpr unsigned L_W1 = 0, L_PT = 0, L_HT = 140 * K;
constexpr unsigned LDS_BYTES = 163840;
constexpr int AUX_SC1 = 16;
constexpr unsigned SPIN_CAP = 1u << 18;
constexpr int W2_A = 4, W2_B = 18;
constexpr unsigned xslot(int i) { return 88 * K + (i & 3) * 16 * K; }

DI f32x4 mfma(v4u a, v4u b, f32x4 c) {
  return __builtin_amdgcn_mfma_f32_16x16x32_bf16(__builtin_bit_cast(bf16x8, a), __builtin_bit_cast(bf16x8, b), c, 0, 0, 0);
}
DI f32x4 rowsum(float v) {
  return __builtin_amdgcn_mfma_f32_16x16x4f32(v, 1.0f, f32x4{0.f, 0.f, 0.f, 0.f}, 0, 0, 0);
}
DI float colsum(float v) {
  return __builtin_amdgcn_mfma_f32_16x16x4f32(1.0f, v, f32x4{0.f, 0.f, 0.f, 0.f}, 0, 0, 0)[0];
}
DI unsigned pack2(float a, float b) {
  bf16x2 v = {(__bf16)a, (__bf16)b};
  return __builtin_bit_cast(unsigned, v);
}
DI float asf(unsigned u) { return __builtin_bit_cast(float, u); }
DI unsigned asu(float f) { return __builtin_bit_cast(unsigned, f); }
DI float bflo(unsigned u) { return asf(u << 16); }
DI float bfhi(unsigned u) { return asf(u & 0xffff0000u); }
DI v4u trfrag2(LAS unsigned char* L, unsigned a, unsigned b) {
  s16x4 lo = __builtin_amdgcn_ds_read_tr16_b64_v4i16((LAS s16x4*)(L + a));
  s16x4 hi = __builtin_amdgcn_ds_read_tr16_b64_v4i16((LAS s16x4*)(L + b));
  v2u l2 = __builtin_bit_cast(v2u, lo), h2 = __builtin_bit_cast(v2u, hi);
  return v4u{l2.x, l2.y, h2.x, h2.y};
}
DI v4u trfrag(LAS unsigned char* L, unsigned a, unsigned dt) { return trfrag2(L, a, a + dt); }
DI void dma16(rsrc_t rs, unsigned voff, unsigned soff, unsigned ldst) {
  asm volatile("s_mov_b32 m0, %0\n\ts_nop 0\n\tbuffer_load_dwordx4 %1, %2, %3 offen lds" ::"s"(ldst), "v"(voff), "s"(rs), "s"(soff) : "memory");
}
DI void dma16s(rsrc_t rs, unsigned voff, unsigned soff, unsigned ldst) {
  asm volatile("s_mov_b32 m0, %0\n\ts_nop 0\n\tbuffer_load_dwordx4 %1, %2, %3 offen sc1 lds" ::"s"(ldst), "v"(voff), "s"(rs), "s"(soff) : "memory");
}
template <int STR> DI void dma16x4(rsrc_t rs, unsigned voff, unsigned soff, unsigned ldst) {
  unsigned t;
  asm volatile("s_mov_b32 m0, %1\n\ts_add_u32 %0, %4, %5\n\tbuffer_load_dwordx4 %2, %3, %4 offen lds\n\t"
               "buffer_load_dwordx4 %2, %3, %0 offen offset:1024 lds\n\ts_add_u32 %0, %0, %5\n\t"
               "buffer_load_dwordx4 %2, %3, %0 offen offset:2048 lds\n\ts_add_u32 %0, %0, %5\n\t"
               "buffer_load_dwordx4 %2, %3, %0 offen offset:3072 lds"
               : "=&s"(t) : "s"(ldst), "v"(voff), "s"(rs), "s"(soff), "i"(STR - 1024) : "memory", "scc");
}
#define BAR() asm volatile("s_waitcnt lgkmcnt(0)\n\ts_barrier" ::: "memory")
#define BAR_VM() asm volatile("s_waitcnt vmcnt(0) lgkmcnt(0)\n\ts_barrier" ::: "memory")
#define DRAIN() asm volatile("s_waitcnt vmcnt(0)" ::: "memory")
#define SB() __builtin_amdgcn_sched_barrier(0)
#define OPQ(x) asm("" : "+v"(x))
constexpr float LOG2E = 1.4426950408889634f;
DI float phi(float t, float rs, float rl) { return __builtin_amdgcn_exp2f(fminf(t * rl, 0.f)) + fmaxf(__builtin_fmaf(t, rs, 1e-6f), 1e-6f); }
constexpr float GC = 0.4686686930532063f, GK = -4.91222953926424f;
DI float gelu_s(float v) {
  float e = __builtin_amdgcn_exp2f(v * __builtin_fmaf(-v, v, GK));
  float y = v * __builtin_amdgcn_rcpf(1.0f + e);
  OPQ(y);
  return y;
}
DI void poll_flags(unsigned char* ws, unsigned off, int idx, u64 tag) {
  const GAS u64* q = (const GAS u64*)(ws + off + idx * FS);
#pragma unroll 1
  for (unsigned it = 0; it < SPIN_CAP; ++it) {
    u64 v = __hip_atomic_load(q, __ATOMIC_RELAXED, __HIP_MEMORY_SCOPE_AGENT);
    if (__all(v == tag)) break;
    __builtin_amdgcn_s_sleep(1);
  }
  __builtin_amdgcn_fence(__ATOMIC_ACQUIRE, "wavefront");
}
DI void set_flag(unsigned char* ws, unsigned off, u64 tag) {
  __hip_atomic_store((GAS u64*)(ws + off), tag, __ATOMIC_RELAXED, __HIP_MEMORY_SCOPE_AGENT);
}
DI void set_flag_line(unsigned char* ws, unsigned off, u64 tag, int t) {
  if (t < 16) set_flag(ws, off + t * 8, tag);
}
DI v4u ld_sc1(rsrc_t rs, unsigned voff, unsigned soff) { return __builtin_amdgcn_raw_buffer_load_b128(rs, voff, soff, AUX_SC1); }
DI v2u ld_sc1_64(rsrc_t rs, unsigned voff, unsigned soff) { return __builtin_amdgcn_raw_buffer_load_b64(rs, voff, soff, AUX_SC1); }
DI void st_sc1(v4u v, rsrc_t rs, unsigned voff, unsigned soff) {
  __builtin_amdgcn_raw_buffer_store_b128(v, rs, voff, soff, AUX_SC1);
  asm volatile("s_nop 1" ::"v"(v) : "memory");
}
DI void st_sc1_64(v2u v, rsrc_t rs, unsigned voff, unsigned soff) { __builtin_amdgcn_raw_buffer_store_b64(v, rs, voff, soff, AUX_SC1); }
DI int wrap(int v, int m) { return v >= m ? v - m : v; }

}

__global__ void __launch_bounds__(512, 2)
fused_block(const u16* __restrict__ x, const u16* __restrict__ wqkv, const u16* __restrict__ wo, const u16* __restrict__ w1,
            const u16* __restrict__ b1, const u16* __restrict__ w2, const u16* __restrict__ b2, u16* __restrict__ out,
            unsigned char* ws) {
  __shared__ __attribute__((aligned(1024))) unsigned char smem[LDS_BYTES];
  LAS unsigned char* L = (LAS unsigned char*)smem;
  const unsigned lbase = (unsigned)(size_t)L;
  const int tid = threadIdx.x, l = tid & 63, w = __builtin_amdgcn_readfirstlane(tid >> 6), g = l >> 4, c = l & 15;
  const int b = blockIdx.x, xcd = b & 7, slot = b >> 3;
  const int i1 = 4 * (xcd >> 1) + (slot >> 3), h = 8 * (xcd & 1) + (slot & 7);
  const int u = 4 * xcd + (slot >> 3), ct = slot & 7;
  const bool act24 = ct < 7;
  int m3 = 8 * i1 + w;
  asm volatile("" : "+s"(m3));
  const int sa = slot >> 3;
  const int r1 = slot % 7;

  const rsrc_t rX = __builtin_amdgcn_make_buffer_rsrc((void*)x, 0, S_ * D_ * 2, 0x00020000);
  const rsrc_t rQ = __builtin_amdgcn_make_buffer_rsrc((void*)wqkv, 0, D_ * 3 * D_ * 2, 0x00020000);
  const rsrc_t rO = __builtin_amdgcn_make_buffer_rsrc((void*)wo, 0, D_ * D_ * 2, 0x00020000);
  const rsrc_t rA = __builtin_amdgcn_make_buffer_rsrc((void*)w1, 0, D_ * F_ * 2, 0x00020000);
  const rsrc_t rB = __builtin_amdgcn_make_buffer_rsrc((void*)w2, 0, F_ * D_ * 2, 0x00020000);
  const rsrc_t rW = __builtin_amdgcn_make_buffer_rsrc((void*)ws, 0, WS_END, 0x00020000);

  const unsigned vW = (l >> 2) * 2688 + (((l >> 1) & 1) ^ (l >> 5)) * 24 + (l & 1) * 16;
  const unsigned vXa = (16 * m3 + (l >> 3)) * 896 + ((l & 7) ^ g) * 16;
  const unsigned vXb = (vXa ^ 64) + 8 * 896;
  const unsigned sW = (32 * (w >> 2) + 16 * (w & 1)) * 2688 + (1 + ((w >> 1) & 1)) * 896 + 56 * h;
  auto issue_w = [&](int i) { dma16(rQ, vW, sW + wrap(i + r1, 7) * 64 * 2688, lbase + L_KV + w * 1024 + i * 8192); };
  auto issue_xa = [&](int i) { dma16(rX, vXa, wrap(i + r1, 7) * 128, lbase + xslot(i) + w * 2048); };
  auto issue_xb = [&](int i) { dma16(rX, vXb, wrap(i + r1, 7) * 128, lbase + xslot(i) + w * 2048 + 1024); };
  auto issue_q = [&](int m) {
    int n = w + 8 * m;
    if (m == 3) n = n >= 28 ? n - 4 : n;
    dma16(rQ, vW, (64 * wrap((n >> 2) + r1, 7) + 16 * (n & 3)) * 2688 + 56 * h, lbase + L_Q + n * 1024);
  };
  auto stage_issue = [&](int i) {
    issue_w(i);
    issue_xa(i);
    issue_xb(i);
  };
  stage_issue(0);
  const unsigned v8 = (l >> 3) * 896 + (((l >> 1) & 3) ^ g) * 32 + (l & 1) * 16 + ct * 128;
  unsigned v1off;
  {
    const int ch = 64 * (w < 7 ? w : 6) + l, row = (ch * 2341) >> 15, cc = ch - 14 * row, js = cc >> 1;
    const int j = (h & 1) ? (js == 6 ? 0 : js + 1) : js;
    v1off = row * 3584 + (112 * h + 16 * j) * 2 + (cc & 1) * 16;
  }
  const int wc = w < 7 ? w : 6;
  const int mt = w & 3, kh = w >> 2;
  unsigned l8 = (l >> 5) * 1024 + ((g & 1) * 4 + (c >> 2)) * 128 + ((g & 1) * 2 + (c >> 3)) * 32 + (c & 3) * 8;
  const int rk1 = i1 < 14 ? i1 : 13, rb2 = w < 4 || b >= 136 ? b : b + 256;
  v4u r2f;
  unsigned cpf = 0;
  v2u ep = {0u, 0u}, ep2 = ep;
  auto aux_loads = [&]() {
    u64 pc;
    unsigned sz;
    asm volatile(".Lfb_start:\n\ts_getpc_b64 %0\n\ts_mov_b32 %1, .Lfb_end-.Lfb_start" : "=s"(pc), "=s"(sz));
    const rsrc_t rC = __builtin_amdgcn_make_buffer_rsrc((void*)pc, 0, sz, 0x00020000);
    asm volatile("s_nop 4\n\tbuffer_load_dword %0, %1, %2, 0 offen" : "=v"(cpf) : "v"(w == 7 ? ((slot & 3) * 64 + l) * 128 : 0x7FFFFFF0), "s"(rC) : "memory");
    const int sel = (g & 1) ? (u >> 1) : (g & 2) ? i1 : h;
    const unsigned f6 = WS_F1 + (g * 256 + sel * 16 + c) * FS, f5 = WS_FL + ((g ? 256 : h * 16) + c) * FS;
    const unsigned fo = w == 6 ? f6 : f5;
    asm volatile("buffer_load_dwordx2 %0, %1, %2, 0 offen sc1" : "=v"(ep) : "v"(w == 5 || w == 6 ? fo : 0u), "s"(rW) : "memory");
    asm volatile("buffer_load_dwordx2 %0, off, %1, 0 sc1" : "=v"(ep2) : "s"(rW) : "memory");
  };
  stage_issue(1);
  issue_w(2);
  issue_w(3);
  issue_xa(2);
  issue_xb(2);
  const unsigned lq0 = (l >> 5) * 1024 + (g & 1) * 544 + (c >> 2) * 64 + (c & 3) * 8, lq1 = lq0 ^ 32;
  unsigned lx0 = xslot(0) + w * 2048 + c * 128 + (g ^ (c >> 1)) * 16, lx1 = lx0 ^ 64;
  OPQ(lx0);
  OPQ(lx1);
  const unsigned xw = L_X + (w >> 1) * 1024 + l * 16 + (w & 1) * 8;
  v4u xf[14];
  f32x4 acc[4], as1 = {0.f, 0.f, 0.f, 0.f}, as2 = as1, acs = as1;
  const unsigned csa = (lq0 ^ ((w & 1) * 32)) + L_KV + (w >> 2) * 4096 + ((w >> 1) & 1) * 2048;
#pragma unroll
  for (int t = 0; t < 4; ++t) acc[t] = f32x4{0.f, 0.f, 0.f, 0.f};
  const unsigned ONES = 0x3F803F80u;
  const v4u on4 = {ONES, ONES, ONES, ONES};
  u64 tag = 0;
  float rs[4], mn[4];
  if (tid == 0) *(LAS v2u*)(L + L_CNT) = v2u{0u, 0u};
  v4u f[2][8], fc[2];
  auto mm = [&](int i, int n) {
    const int ss = n / 6, r = n % 6;
    if (n == 12) acs = mfma(on4, fc[i & 1], acs);
    else if (r == 0) as1 = mfma(xf[2 * i + ss], on4, as1);
    else if (r == 1) as2 = mfma(xf[2 * i + ss], xf[2 * i + ss], as2);
    else acc[r - 2] = mfma(xf[2 * i + ss], f[i & 1][4 * ss + r - 2], acc[r - 2]);
  };
  auto rd = [&](int i, int n) {
    if (n == 0) xf[2 * i] = *(LAS v4u*)(L + (i & 3) * 16 * K + lx0);
    else if (n == 5) xf[2 * i + 1] = *(LAS v4u*)(L + (i & 3) * 16 * K + lx1);
    else if (n == 10) fc[i & 1] = trfrag(L, csa + i * 8192, 256);
    else {
      const int q = n < 5 ? n - 1 : n - 2;
      f[i & 1][q] = trfrag(L, ((q & 1) ? lq1 : lq0) + L_KV + (2 * i + (q >> 2)) * 4096 + ((q >> 1) & 1) * 2048, 256);
    }
  };
  float cs0[4];
  v4u g2a, fc6;
#pragma unroll
  for (int i = 0; i < 7; ++i) {
    constexpr int NW[7] = {7, 10, 9, 9, 7, 6, 4};
    asm volatile("s_waitcnt vmcnt(%3) lgkmcnt(0)\n\ts_barrier" : "+v"(ep), "+v"(ep2), "+v"(cpf) : "i"(NW[i]) : "memory");
#pragma unroll
    for (int n = 0; n < 13; ++n) {
      if (n < (i < 6 ? 11 : 10)) rd(i, n);
      if (i > 0 && i < 6) mm(i - 1, n);
      if (i == 0) {
        if (n == 5) issue_w(4);
        if (n == 6) issue_xa(3);
        if (n == 7) issue_xb(3);
        if (n == 8) aux_loads();
      }
      if (i == 1 || i == 2) {
        if (n == 1) issue_w(i + 4);
        if (n == 5) issue_xa(i + 3);
        if (n == 9) issue_xb(i + 3);
      }
      if (i == 3) {
        if (n == 1) issue_xa(6);
        if (n == 4) issue_xb(6);
        if (n == 7) dma16(rA, v1off, rk1 * 32 * 3584, lbase + L_RP + 8 * K + wc * 1024);
        if (n == 10) dma16(rB, v8 - ct * 128, (32 * (rb2 % 56) + 8 * (w & 3)) * 896 + (rb2 / 56) * 128, lbase + L_RP + w * 1024);
      }
      if (i == 4 || i == 5) {
        if (n == 1) issue_q(2 * i - 8);
        if (n == 7) issue_q(2 * i - 7);
      }
      if (i == 5 && n == 11) fc6 = trfrag(L, csa + 6 * 8192, 256);
      if (i == 6 && n >= 4 && n < 12) mm(6, n < 8 ? (n & 1) + 6 * ((n - 4) >> 1) : n - 6);
      SB();
    }
    if (i == 5) {
      mm(5, 12);
      acs = mfma(on4, fc6, acs);
#pragma unroll
      for (int n = 0; n < 6; ++n) mm(5, n);
      if (g == 0) *(LAS float*)(L + L_CS1 + w * 64 + c * 4) = acs[0];
#pragma unroll
      for (int n = 6; n < 12; ++n) mm(5, n);
    }
    if (i == 4) {
      const u64 e = ((u64)(unsigned)__builtin_amdgcn_readfirstlane(ep2.y) << 32) | (unsigned)__builtin_amdgcn_readfirstlane(ep2.x);
      tag = e * 6364136223846793005ull + 1442695040888963407ull;
    }
    SB();
  }
  float dg1, dg2;
  {
#pragma unroll
    for (int t = 0; t < 4; ++t) cs0[t] = *(LAS float*)(L + L_CS1 + t * 64 + c * 4) + *(LAS float*)(L + L_CS1 + (t + 4) * 64 + c * 4);
    {
      const unsigned ra = ((l >> 5) * 8 + (g & 1) * 4 + (c >> 2)) * 224 + (c & 3) * 8 + L_RP + 8 * K;
      g2a = trfrag(L, ra + wc * 32, 3584);
      r2f = trfrag(L, (l8 ^ ((w & 3) * 32)) + L_RP + (w >> 2) * 4096, 2048);
    }
    const bool b0 = c & 1, b1 = c & 2, dg = g == (c >> 2);
    dg2 = b1 ? (b0 ? as2[3] : as2[2]) : (b0 ? as2[1] : as2[0]);
    dg2 = dg ? dg2 : 0.f;
    const f32x4 a2 = rowsum(dg2);
    SB();
#pragma unroll
    for (int n = 8; n < 12; ++n) mm(6, n);
#pragma unroll
    for (int r = 0; r < 4; ++r) {
      mn[r] = as1[r] * (-1.0f / 448.0f);
      rs[r] = __builtin_amdgcn_rsqf(fmaxf(__builtin_fmaf(a2[r], 1.0f / 448.0f, -mn[r] * mn[r]), 0.f) + 1e-5f);
    }
    dg1 = b1 ? (b0 ? as1[3] : as1[2]) : (b0 ? as1[1] : as1[0]);
    dg1 = dg ? dg1 : 0.f;
  }
#pragma unroll
  for (int t = 0; t < 4; ++t) {
    float v[4];
#pragma unroll
    for (int r = 0; r < 4; ++r) {
      const float d = __builtin_fmaf(mn[r], cs0[t], acc[t][r]);
      v[r] = t < 2 ? phi(d, rs[r], rs[r] * LOG2E) : d * rs[r];
      if (t == 3) v[r] = c < 4 ? 1.0f : v[r];
      OPQ(v[r]);
    }
    *(LAS v2u*)(L + xw + t * 4096) = v2u{pack2(v[0], v[1]), pack2(v[2], v[3])};
  }
  BAR_VM();
  if (w < 4) {
    f32x4 kv = {0.f, 0.f, 0.f, 0.f};
    v4u a[4], bb[4];
#pragma unroll
    for (int ks = 0; ks < 4; ++ks) {
      a[ks] = *(LAS v4u*)(L + L_X + (((w >> 1) * 4 + ks) * 64 + l) * 16);
      bb[ks] = *(LAS v4u*)(L + L_X + (((2 + (w & 1)) * 4 + ks) * 64 + l) * 16);
    }
#pragma unroll
    for (int ks = 0; ks < 4; ++ks) kv = mfma(a[ks], bb[ks], kv);
    if (w >= 2 && g == 0) kv = f32x4{0.f, 0.f, 0.f, 0.f};
    st_sc1(__builtin_bit_cast(v4u, kv), rW, l * 16, WS_KVP + ((h * 4 + w) * 16 + i1) * 1024);
  }
  v4u pqf;
  v2u xr0, xr1, bb1;
  {
    constexpr int QB = 8;
    f32x4 aq[2] = {f32x4{0.f, 0.f, 0.f, 0.f}, f32x4{0.f, 0.f, 0.f, 0.f}};
    f32x4 csq = {0.f, 0.f, 0.f, 0.f};
    unsigned lqa = lq0 + L_Q, lqb = lq1 + L_Q;
    OPQ(lqa);
    OPQ(lqb);
    const unsigned qa = (lqa ^ ((w & 1) * 32)) + (w >> 1) * 2048;
    v4u f[28], fc[4];
#pragma unroll
    for (int q = 0; q < 6; ++q) f[q] = trfrag(L, ((q & 1) ? lqb : lqa) + (q >> 1) * 2048, 256);
    SB();
    const unsigned on3 = w < 4 ? ONES : 0u;
    const v4u on43 = {on3, on3, on3, on3};
    unsigned kvo[4];
#pragma unroll
    for (int n = 0; n < 4; ++n) kvo[n] = WS_KVP + ((h * 4 + (w & 3)) * 16 + 8 * (w >> 2) + 2 * n) * 1024;
    u64 early = 0;
#pragma unroll
    for (int q = 0; q < 28; ++q) {
      if (q + 6 < 28) f[q + 6] = trfrag(L, ((q & 1) ? lqb : lqa) + ((q + 6) >> 1) * 2048, 256);
      if (q < 4) fc[q] = trfrag(L, qa + (q < 3 ? q * 8192 : (w < 4 ? 3 * 8192 : 0)), 256);
      aq[q & 1] = mfma(f[q], xf[q >> 1], aq[q & 1]);
      if (q >= 6 && q < 10) csq = mfma(q < 9 ? on4 : on43, fc[q - 6], csq);
      if (q == QB && w < 4) {
        DRAIN();
        unsigned old = 0;
        if (l == 0) old = __hip_atomic_fetch_add((LAS unsigned*)(L + L_CNT), 1u, __ATOMIC_RELAXED, __HIP_MEMORY_SCOPE_WORKGROUP);
        if (__builtin_amdgcn_readfirstlane(old) == 3u) set_flag_line(ws, WS_F1 + (h * 16 + i1) * FS, tag, l);
      }
      if (q % 4 == 0 && q < 20 && w >= 4 && w < 7) {
        const int gi = wrap((w - 4) * 5 + q / 4 + 4 * sa, 14);
        if (w < 6 || q < 16) dma16x4<8 * 896>(rO, v8, gi * 32 * 896, lbase + L_WO + gi * 4096);
      }
      if (q == 18 && g == 0) *(LAS float*)(L + L_CSQ + w * 64 + c * 4) = csq[0];
      if (q == 24 && w == 7) early = __hip_atomic_load((const GAS u64*)(ws + WS_F1 + (h * 16 + c) * FS), __ATOMIC_RELAXED, __HIP_MEMORY_SCOPE_AGENT);
      SB();
    }
    auto rstore = [&]() {
      st_sc1(g2a, rW, l * 16, WS_R1 + ((h * 14 + rk1) * 7 + wc) * 1024);
      st_sc1(r2f, rW, l * 16, WS_R2 + (rb2 * 4 + (w & 3)) * 1024);
    };
    if (w < 7) rstore();
    const float mq = colsum(dg1) * (-1.0f / 448.0f);
    const float rq = __builtin_amdgcn_rsqf(fmaxf(__builtin_fmaf(colsum(dg2), 1.0f / 448.0f, -mq * mq), 0.f) + 1e-5f);
    if (w == 7 && !__all(early == tag)) poll_flags(ws, WS_F1 + h * 16 * FS, c, tag);
    BAR();
    v4u p[8];
#pragma unroll
    for (int n = 0; n < 8; ++n) {
      p[n] = ld_sc1(rW, l * 16 + (n & 1) * 1024, kvo[n >> 1]);
      SB();
    }
    if (w == 7) rstore();
    const unsigned vxr = (64 * u + 16 * mt + c) * 896 + (64 * ct + 32 * kh + 4 * g) * 2;
    xr0 = __builtin_amdgcn_raw_buffer_load_b64(rX, vxr, 0, 0);
    xr1 = __builtin_amdgcn_raw_buffer_load_b64(rX, vxr + 32, 0, 0);
    bb1 = *(const GAS v2u*)(b1 + 112 * h + 16 * ((h & 1) ? (wc == 6 ? 0 : wc + 1) : wc) + 4 * g);
    SB();
#pragma unroll
    for (int t = 0; t < 2; ++t) {
      f32x4 cs = *(LAS f32x4*)(L + L_CSQ + t * 64 + g * 16);
#pragma unroll
      for (int m = 1; m < 4; ++m) cs += *(LAS f32x4*)(L + L_CSQ + (t + 2 * m) * 64 + g * 16);
      float v[4];
#pragma unroll
      for (int r = 0; r < 4; ++r) v[r] = phi(__builtin_fmaf(mq, cs[r], aq[t][r]), rq, rq * LOG2E);
      pqf[2 * t] = pack2(v[0], v[1]);
      pqf[2 * t + 1] = pack2(v[2], v[3]);
    }
    asm volatile("" : "+v"(pqf));
    SB();
    f32x4 sum = __builtin_bit_cast(f32x4, p[0]);
#pragma unroll
    for (int n = 1; n < 8; ++n) sum += __builtin_bit_cast(f32x4, p[n]);
    *(LAS f32x4*)(L + L_KVS + w * 1024 + l * 16) = sum;
  }
  BAR_VM();
  set_flag_line(ws, WS_FL + (h * 16 + i1) * FS, tag, tid);
  if (tid == 64) set_flag(ws, WS_FR + (h * 16 + i1) * 8, tag);
  {
    f32x4 T[4], Z[2];
#pragma unroll
    for (int dt = 0; dt < 2; ++dt)
      Z[dt] = *(LAS f32x4*)(L + L_KVS + (dt * 2 + 1) * 1024 + g * 256) + *(LAS f32x4*)(L + L_KVS + (dt * 2 + 5) * 1024 + g * 256);
#pragma unroll
    for (int t = 0; t < 4; ++t) T[t] = *(LAS f32x4*)(L + L_KVS + t * 1024 + l * 16) + *(LAS f32x4*)(L + L_KVS + (t + 4) * 1024 + l * 16);
    v4u f0 = {pack2(T[0].x, T[0].y), pack2(T[0].z, T[0].w), pack2(T[2].x, T[2].y), pack2(T[2].z, T[2].w)};
    v4u f1 = {pack2(T[1].x, T[1].y), pack2(T[1].z, T[1].w), pack2(T[3].x, T[3].y), pack2(T[3].z, T[3].w)};
    v4u fz = {pack2(Z[0].x, Z[0].y), pack2(Z[0].z, Z[0].w), pack2(Z[1].x, Z[1].y), pack2(Z[1].z, Z[1].w)};
    const f32x4 z4 = {0.f, 0.f, 0.f, 0.f};
    f32x4 den = mfma(fz, pqf, z4), n0 = mfma(f0, pqf, z4), n1 = mfma(f1, pqf, z4);
    const float rd = __builtin_amdgcn_rcpf(den.x + 1e-6f);
    const float rd1 = g == 0 ? 0.f : rd;
    v4u of = {pack2(n0.x * rd, n0.y * rd), pack2(n0.z * rd, n0.w * rd), pack2(n1.x * rd1, n1.y * rd1), pack2(n1.z * rd1, n1.w * rd1)};
    st_sc1(of, rW, l * 16, WS_O + (m3 * 16 + h) * 1024);
  }
  u64 earlyr = 0;
  if (w == 6) earlyr = __hip_atomic_load((const GAS u64*)(ws + WS_FL + (h * 16 + c) * FS), __ATOMIC_RELAXED, __HIP_MEMORY_SCOPE_AGENT);
  if (w == 6 && __all(earlyr == tag) && l == 0) *(volatile LAS unsigned*)(L + L_CNT + 4) = 1u;
  BAR_VM();
  set_flag_line(ws, WS_F2 + (i1 * 16 + h) * FS, tag, tid);
  asm volatile("" : "+v"(xr0), "+v"(xr1), "+v"(bb1));
  unsigned w1A = WS_R1 + (h * 98 + w) * 1024, w1D = lbase + L_W1 + w * 1024;
  asm volatile("" : "+s"(w1A), "+s"(w1D));
  auto w1dma = [&](int s) { dma16s(rW, l * 16, w1A + s * 7168, w1D + s * 7168); };
  constexpr int W1A = 8;
  auto w1early = [&]() {
    if (w < 7) {
      if (w == 6 && !__all(earlyr == tag)) {
        poll_flags(ws, WS_FL + (h * 16 + c) * FS, 0, tag);
        if (l == 0) *(volatile LAS unsigned*)(L + L_CNT + 4) = 1u;
      }
#pragma unroll 1
      for (unsigned it = 0; it < (1u << 22) && __builtin_amdgcn_readfirstlane(*(volatile LAS unsigned*)(L + L_CNT + 4)) == 0; ++it) __builtin_amdgcn_s_sleep(1);
      asm volatile("" ::: "memory");
#pragma unroll
      for (int s = 0; s < W1A; ++s) w1dma(s);
    }
  };
  v4u hres;
  if (act24) {
    unsigned wb[4];
#pragma unroll
    for (int j = 0; j < 4; ++j) wb[j] = (l8 ^ (kh * 64) ^ (j * 32)) + L_WO + kh * (224 * 128);
    auto wfrag = [&](int q) {
      const int n = q >> 2, jl = q & 3;
      const unsigned po = n * (28 * 128);
      return trfrag2(L, wb[jl ^ ((n & 1) * 2)] + po, wb[jl ^ (((n & 1) ^ 1) * 2)] + po + 12 * 128);
    };
    unsigned oo[4];
#pragma unroll
    for (int n = 0; n < 4; ++n) {
      oo[n] = WS_O + ((4 * u + mt) * 16 + 8 * kh + 2 * n) * 1024;
      asm volatile("" : "+s"(oo[n]));
    }
    v4u wf[32];
#pragma unroll
    for (int q = 0; q < 32; ++q) {
      wf[q] = wfrag(q);
      if (q == 0) {
        SB();
        w1early();
        SB();
      }
    }
    const int m2 = 4 * u + mt;
    f32x4 a2[4] = {f32x4{bflo(xr0.x), bfhi(xr0.x), bflo(xr0.y), bfhi(xr0.y)}, f32x4{bflo(xr1.x), bfhi(xr1.x), bflo(xr1.y), bfhi(xr1.y)},
                   f32x4{0.f, 0.f, 0.f, 0.f}, f32x4{0.f, 0.f, 0.f, 0.f}};
    asm volatile("" : "+v"(a2[0]), "+v"(a2[1]));
    if (w == 7) {
      const GAS u64* q = (const GAS u64*)(ws + WS_F2 + ((u >> 1) * 16 + c) * FS);
      const unsigned tl = (unsigned)tag, th = (unsigned)(tag >> 32);
#pragma unroll 1
      for (unsigned it = 0; it < SPIN_CAP; ++it) {
        const u64 v = __hip_atomic_load(q, __ATOMIC_RELAXED, __HIP_MEMORY_SCOPE_AGENT);
        const v4u fa = ld_sc1(rW, l * 32, WS_FR), fb = ld_sc1(rW, l * 32 + 16, WS_FR);
        if (__all(v == tag && fa.x == tl && fa.y == th && fa.z == tl && fa.w == th && fb.x == tl && fb.y == th && fb.z == tl && fb.w == th)) break;
        asm volatile("s_sleep 1" ::: "memory");
      }
    }
    BAR();
    v4u of[8];
    auto oload = [&](int n) { of[n] = ld_sc1(rW, l * 16 + (n & 1) * 1024, oo[n >> 1]); };
#pragma unroll
    for (int n = 0; n < 8; ++n) {
      oload(n);
      SB();
    }
#pragma unroll
    for (int q = 0; q < 32; ++q) {
      if (q == 12) asm volatile("" : "+v"(of[3]), "+v"(of[4]), "+v"(of[5]), "+v"(of[6]), "+v"(of[7]));
      if (q >= 12 && q % 2 == 0 && W1A + (q - 12) / 2 < 14 && w < 7) w1dma(W1A + (q - 12) / 2);
      a2[q & 3] = mfma(wf[q], of[q >> 2], a2[q & 3]);
      SB();
    }
    *(LAS f32x4*)(L + L_EX + w * 2048 + l * 16) = a2[2];
    *(LAS f32x4*)(L + L_EX + w * 2048 + 1024 + l * 16) = a2[3];
    BAR();
    a2[0] += *(LAS f32x4*)(L + L_EX + (w ^ 4) * 2048 + l * 16);
    a2[1] += *(LAS f32x4*)(L + L_EX + (w ^ 4) * 2048 + 1024 + l * 16);
    hres = v4u{pack2(a2[0].x, a2[0].y), pack2(a2[0].z, a2[0].w), pack2(a2[1].x, a2[1].y), pack2(a2[1].z, a2[1].w)};
    st_sc1(hres, rW, l * 16, WS_H + (m2 * 16 + 2 * ct + kh) * 1024);
    const f32x4 z4 = {0.f, 0.f, 0.f, 0.f};
    const f32x4 gm = mfma(hres, hres, z4);
    const float t1 = mfma(on4, hres, z4)[0];
    const float t2 = (c & 2) ? ((c & 1) ? gm[3] : gm[2]) : ((c & 1) ? gm[1] : gm[0]);
    if (g == (c >> 2)) st_sc1_64(v2u{asu(t1), asu(t2)}, rW, c * 8, WS_ST + (m2 * 16 + 2 * ct + kh) * 128);
    BAR_VM();
    set_flag_line(ws, WS_F3 + (u * 8 + ct) * FS, tag, tid);
    if (b == 0 && tid == 64) set_flag(ws, WS_EPOCH, tag);
  } else {
    w1early();
    if (w < 7) {
#pragma unroll
      for (int s = W1A; s < 14; ++s) w1dma(s);
    }
    BAR_VM();
  }
  v4u wf2[7][4];
  unsigned w2o[7];
#pragma unroll
  for (int kk = 0; kk < 7; ++kk) {
    w2o[kk] = WS_R2 + (ct * 56 + 7 * w + kk) * 4096;
    asm volatile("" : "+s"(w2o[kk]));
  }
  auto w2load = [&](int i) { wf2[i >> 2][i & 3] = ld_sc1(rW, l * 16 + (i & 3) * 1024, w2o[i >> 2]); };
  unsigned lP3 = L_W1 + l * 16, lP3b = lP3 + 7 * 7168;
  OPQ(lP3b);
  {
    v4u f[14];
#pragma unroll
    for (int s = 0; s < 14; ++s) f[s] = *(LAS v4u*)(L + (s < 7 ? lP3 + s * 7168 : lP3b + (s - 7) * 7168) + wc * 1024);
    SB();
    f32x4 cs = {0.f, 0.f, 0.f, 0.f};
#pragma unroll
    for (int s = 0; s < 14; ++s) {
      cs = mfma(f[s], on4, cs);
      if (s < W2_A) w2load(s);
      SB();
    }
    if (c == 0) {
      *(LAS f32x4*)(L + L_C1 + wc * 64 + g * 16) = cs * GC;
      *(LAS f32x4*)(L + L_C1 + 448 + wc * 64 + g * 16) = f32x4{bflo(bb1.x), bfhi(bb1.x), bflo(bb1.y), bfhi(bb1.y)} * GC;
    }
  }
  {
    auto fs = [](int q) { return q < 56 ? q >> 2 : (q - 56) % 14; };
    auto fj = [](int q) { return q < 56 ? q & 3 : 4 + (q - 56) / 14; };
    auto frag = [&](int q) {
      const int s = fs(q), j = fj(q);
      return *(LAS v4u*)(L + (s < 7 ? lP3 + s * 7168 : lP3b + (s - 7) * 7168) + j * 1024);
    };
    constexpr int PF3 = 6;
    v4u f[98];
#pragma unroll
    for (int q = 0; q < PF3; ++q) f[q] = frag(q);
    unsigned ho[14];
#pragma unroll
    for (int s = 0; s < 14; ++s) {
      ho[s] = WS_H + (m3 * 16 + s) * 1024;
      asm volatile("" : "+s"(ho[s]));
    }
    if (w == 7) poll_flags(ws, WS_F3 + i1 * 16 * FS, (c & 7) == 7 ? 0 : c, tag);
    BAR();
    v4u hf[14];
    auto hld = [&](int s) { hf[s] = ld_sc1(rW, l * 16, ho[s]); };
    constexpr int HA = 14;
#pragma unroll
    for (int s = 0; s < HA; ++s) {
      hld(s);
      SB();
    }
    v2u st[4];
    f32x4 a3[7];
#pragma unroll
    for (int j = 0; j < 7; ++j) a3[j] = f32x4{0.f, 0.f, 0.f, 0.f};
#pragma unroll
    for (int q = 0; q < 56; ++q) {
      f[q + PF3] = frag(q + PF3);
      a3[fj(q)] = mfma(f[q], hf[fs(q)], a3[fj(q)]);
      if (q >= 8 && q < 12) st[q - 8] = ld_sc1_64(rW, c * 8 + g * 128 + (q - 8) * 512, WS_ST + m3 * 2048);
      if (q >= 32 && q % 4 == 0) w2load(W2_A + (q - 32) / 4);
      SB();
    }
    float t1 = asf(st[0].x) + asf(st[1].x) + asf(st[2].x) + (g < 2 ? asf(st[3].x) : 0.f);
    float t2 = asf(st[0].y) + asf(st[1].y) + asf(st[2].y) + (g < 2 ? asf(st[3].y) : 0.f);
    const float mean2 = colsum(t1) * (1.0f / 448.0f);
    const float rs2 = __builtin_amdgcn_rsqf(fmaxf(__builtin_fmaf(colsum(t2), 1.0f / 448.0f, -mean2 * mean2), 0.f) + 1e-5f);
    const float nm2 = -mean2 * rs2, rs2c = rs2 * GC;
    const unsigned ksp = (7 * h + 1) >> 1;
    const unsigned ob = WS_H1 + (m3 * 56 + ksp) * 1024;
    unsigned pk[14];
    auto epi1 = [&](int j, int r, const f32x4& cs, const f32x4& bb) {
      return gelu_s(__builtin_fmaf(a3[j][r], rs2c, __builtin_fmaf(nm2, cs[r], bb[r])));
    };
#pragma unroll
    for (int jj = 0; jj < 7; ++jj) {
      f32x4 cs = *(LAS f32x4*)(L + L_C1 + jj * 64 + g * 16);
      f32x4 bb = *(LAS f32x4*)(L + L_C1 + 448 + jj * 64 + g * 16);
      SB();
      float y[4];
#pragma unroll
      for (int s = 0; s < 7; ++s) {
        const int q = 56 + jj * 7 + s;
        if (jj < 6) {
          if (q + PF3 < 98) f[q + PF3] = frag(q + PF3);
          a3[fj(q)] = mfma(f[q], hf[fs(q)], a3[fj(q)]);
          if ((s == 1 || s == 4) && 6 + jj * 2 + s / 4 < W2_B) w2load(W2_A + 6 + jj * 2 + s / 4);
        }
        if (s == 2 || s == 4) {
          y[s - 2] = epi1(jj, s - 2, cs, bb);
          y[s - 1] = epi1(jj, s - 1, cs, bb);
        }
        if (s == 3 || s == 5) pk[2 * jj + (s - 3) / 2] = pack2(y[s - 3], y[s - 2]);
        if (s != 2 && s != 4) SB();
      }
      if (jj & 1) st_sc1(v4u{pk[2 * jj - 2], pk[2 * jj - 1], pk[2 * jj], pk[2 * jj + 1]}, rW, l * 16 + (jj >> 1) * 1024, ob);
    }
    st_sc1_64(v2u{pk[12], pk[13]}, rW, l * 8, (h & 1) ? ob - 512 : ob + 3072);
    BAR_VM();
    set_flag_line(ws, WS_F4 + (i1 * 16 + h) * FS, tag, tid);
  }
  if (act24) {
#pragma unroll
    for (int i = W2_A + W2_B; i < 28; ++i) w2load(i);
    v4u bb2 = *(const GAS v4u*)(b2 + 64 * ct + (tid & 7) * 8);
    *(LAS v2u*)(L + L_HT + (16 * mt + c) * 144 + (32 * kh + 4 * g) * 2) = v2u{hres.x, hres.y};
    *(LAS v2u*)(L + L_HT + (16 * mt + c) * 144 + (32 * kh + 16 + 4 * g) * 2) = v2u{hres.z, hres.w};
    unsigned ko[7], mo[4];
#pragma unroll
    for (int kk = 0; kk < 7; ++kk) {
      ko[kk] = l * 16 + kk * 1024;
      asm volatile("" : "+v"(ko[kk]));
    }
#pragma unroll
    for (int m = 0; m < 4; ++m) {
      mo[m] = WS_H1 + ((4 * u + m) * 56 + 7 * w) * 1024;
      asm volatile("" : "+s"(mo[m]));
    }
    if (w == 7) poll_flags(ws, WS_F4 + (u >> 1) * 16 * FS, c, tag);
    BAR();
    v4u hf[28];
    auto hload = [&](int i) {
      const int m = i / 7, kk = i % 7;
      if (kk == 3) {
        const v2u lo = ld_sc1_64(rW, l * 8 + 3072, mo[m]), hi = ld_sc1_64(rW, l * 8 + 3584, mo[m]);
        hf[i] = v4u{lo.x, lo.y, hi.x, hi.y};
      } else {
        hf[i] = ld_sc1(rW, ko[kk], mo[m]);
      }
    };
    constexpr int HB = 10;
#pragma unroll
    for (int i = 0; i < HB; ++i) {
      hload(i);
      SB();
    }
    f32x4 a4[4][4];
    unsigned pw0 = L_PT + w * 16384 + c * 256 + (g ^ (c & 7)) * 16, pw1 = pw0 ^ 64;
    OPQ(pw0);
    OPQ(pw1);
    auto ptw = [&](int m, int j) { *(LAS f32x4*)(L + ((j & 1) ? pw1 : pw0) + m * 4096 + (j >> 1) * 128) = a4[m][j]; };
    constexpr float IG = 1.0f / GC;
#pragma unroll
    for (int m = 0; m < 4; ++m) {
#pragma unroll
      for (int j = 0; j < 4; ++j) a4[m][j] = f32x4{0.f, 0.f, 0.f, 0.f};
#pragma unroll
      for (int kk = 0; kk < 7; ++kk) {
        if (7 * m + kk + HB < 28) hload(7 * m + kk + HB);
        if (m > 0 && kk >= 1 && kk <= 4) ptw(m - 1, kk - 1);
#pragma unroll
        for (int j = 0; j < 4; ++j) a4[m][j] = mfma(wf2[kk][j], hf[7 * m + kk], a4[m][j]);
        SB();
      }
    }
    unsigned pa = L_PT + (tid >> 3) * 256 + (((tid & 7) * 2) ^ ((tid >> 3) & 7)) * 16, pb = pa + 65536;
    OPQ(pb);
    const v4u hh = *(LAS v4u*)(L + L_HT + (tid >> 3) * 144 + (tid & 7) * 16);
    f32x4 e0 = {bflo(bb2.x) + bflo(hh.x), bfhi(bb2.x) + bfhi(hh.x), bflo(bb2.y) + bflo(hh.y), bfhi(bb2.y) + bfhi(hh.y)};
    f32x4 e1 = {bflo(bb2.z) + bflo(hh.z), bfhi(bb2.z) + bfhi(hh.z), bflo(bb2.w) + bflo(hh.w), bfhi(bb2.w) + bfhi(hh.w)};
    GAS v4u* op = (GAS v4u*)(out + (64 * u + (tid >> 3)) * 448 + 64 * ct + (tid & 7) * 8);
    asm volatile("" : "+v"(e0), "+v"(e1), "+v"(op));
#pragma unroll
    for (int j = 0; j < 4; ++j) ptw(3, j);
    BAR();
    f32x4 r0[8], r1_[8];
#pragma unroll
    for (int ww = 0; ww < 8; ++ww) {
      r0[ww] = *(LAS f32x4*)(L + (ww < 4 ? pa : pb) + (ww & 3) * 16384);
      r1_[ww] = *(LAS f32x4*)(L + ((ww < 4 ? pa : pb) ^ 16) + (ww & 3) * 16384);
    }
    SB();
    const f32x4 o0 = ((r0[0] + r0[1]) + (r0[2] + r0[3])) + ((r0[4] + r0[5]) + (r0[6] + r0[7]));
    const f32x4 o1 = ((r1_[0] + r1_[1]) + (r1_[2] + r1_[3])) + ((r1_[4] + r1_[5]) + (r1_[6] + r1_[7]));
    e0 += o0 * IG;
    e1 += o1 * IG;
    *op = v4u{pack2(e0.x, e0.y), pack2(e0.z, e0.w), pack2(e1.x, e1.y), pack2(e1.z, e1.w)};
  }
  asm volatile(".Lfb_end:" ::: "memory");
}

extern "C" void kernel_launch(void* const* d_in, const int* in_sizes, int n_in, void* d_out, int out_size, void* d_ws,
                              size_t ws_size, hipStream_t stream) {
  (void)in_sizes; (void)n_in; (void)out_size; (void)ws_size;
  fused_block<<<256, 512, 0, stream>>>((const u16*)d_in[0], (const u16*)d_in[1], (const u16*)d_in[2], (const u16*)d_in[4],
                                       (const u16*)d_in[5], (const u16*)d_in[6], (const u16*)d_in[7], (u16*)d_out,
                                       (unsigned char*)d_ws);
}
```
